```python
import torch

def _softmax(x, axis=-1):
    return torch.softmax(x, dim=axis)
def _gelu(x):
    return torch.nn.functional.gelu(x, approximate="tanh")
def _silu(x):
    return torch.nn.functional.silu(x)
def _sigmoid(x):
    return torch.sigmoid(x)
def _layernorm(x, axis=-1, eps=1e-5):
    m = x.mean(dim=axis, keepdim=True)
    v = x.var(dim=axis, keepdim=True, unbiased=False)
    return (x - m) / torch.sqrt(v + eps)
def _scatter_add(tgt, idx, src, axis=0):
    return tgt.clone().index_add_(axis, idx, src)
def _segment_sum(src, seg, n):
    out = torch.zeros((n,) + src.shape[1:], dtype=src.dtype)
    return out.index_add_(0, seg, src)
def _pad(x, pw):
    flat = tuple(v for pair in reversed(pw) for v in pair)
    return torch.nn.functional.pad(x, flat)

kernel_name = 'recurrent_scan__sliding_block__dense_activation_relu__prompt'

def setup_inputs():

    _w = (lambda a: (lambda t: t.to(torch.bfloat16) if t.is_floating_point() else t)(torch.as_tensor(a)))
    def _A_setup():
        B, S, D = 1, 2048, 640
        return {
            "x": _w((lambda *s: torch.randn(*s, dtype=torch.bfloat16) * 0.05)(B, S, D)),
            "wqkv": _w((lambda *s: torch.randn(*s, dtype=torch.bfloat16) * 0.05)(D, 3 * D)),
            "wo": _w((lambda *s: torch.randn(*s, dtype=torch.bfloat16) * 0.05)(D, D)),
        }
    _A_in = _A_setup()

    def _B_setup():
        B, S, D, Dff = 1, 2048, 640, 1536
        return {
            "x": _w((lambda *s: torch.randn(*s, dtype=torch.bfloat16) * 0.05)(B, S, D)),
            "w1": _w((lambda *s: torch.randn(*s, dtype=torch.bfloat16) * 0.05)(D, Dff)),
            "w2": _w((lambda *s: torch.randn(*s, dtype=torch.bfloat16) * 0.05)(Dff, D)),
        }
    _B_in = _B_setup()

    return {
        "A_x": _A_in["x"],
        "A_wqkv": _A_in["wqkv"],
        "A_wo": _A_in["wo"],
        "B_x": _B_in["x"],
        "B_w1": _B_in["w1"],
        "B_w2": _B_in["w2"],
    }

def reference(A_x, A_wqkv, A_wo, B_x, B_w1, B_w2):
    def _A_ref(x, wqkv, wo):
        B, S, D = x.shape
        H = 32; Dh = D // H; W = 256
        qkv = (lambda x, *d: x.permute(*d))((x @ wqkv).reshape(B, S, 3, H, Dh), 2, 0, 3, 1, 4)
        q, k, v = qkv[0], qkv[1], qkv[2]
        scores = torch.einsum("bhqd,bhkd->bhqk", q, k) * (Dh ** -0.5)
        idx = torch.arange(S)
        mask = (idx[:, None] - idx[None, :] >= 0) & (idx[:, None] - idx[None, :] < W)
        scores = torch.where(mask[None, None], scores, -1e9)
        p = _softmax(scores, axis=-1)
        o = torch.einsum("bhqk,bhkd->bhqd", p, v)
        return (lambda x, *d: x.permute(*d))(o, 0, 2, 1, 3).reshape(B, S, D) @ wo

    def _B_ref(x, w1, w2):
        h = torch.einsum("bsd,df->bsf", x, w1)
        zero = torch.zeros_like(h)
        h = torch.maximum(h, zero)
        return torch.einsum("bsf,fd->bsd", h, w2)

    x = A_x
    xn = _layernorm(x, axis=-1)
    s = _A_ref(xn, A_wqkv, A_wo)
    g = _silu(_B_ref(xn, B_w1, B_w2))
    return x + s * g
```

```cpp
#include <hip/hip_runtime.h>
#include <hip/hip_bf16.h>


typedef unsigned short u16;
using bf16x8 = __attribute__((ext_vector_type(8))) short;
using s16x4  = __attribute__((ext_vector_type(4))) short;
using f32x4  = __attribute__((ext_vector_type(4))) float;
using u32x4  = __attribute__((ext_vector_type(4))) unsigned;
using u32x2  = __attribute__((ext_vector_type(2))) unsigned;
typedef __attribute__((address_space(3))) s16x4 lds_s16x4;

constexpr int S_ = 2048, D_ = 640, H_ = 32, DH_ = 20, FF_ = 1536, NQKV_ = 1920;

__device__ __forceinline__ float bf2f(u16 v) { return __uint_as_float(((unsigned)v) << 16); }
__device__ __forceinline__ u16 f2bf(float f) {
  __bf16 b = (__bf16)f;
  return __builtin_bit_cast(u16, b);
}
__device__ __forceinline__ float rbf(float f) { return bf2f(f2bf(f)); }
__device__ __forceinline__ unsigned pack2(float a, float b) {
  return (unsigned)f2bf(a) | ((unsigned)f2bf(b) << 16);
}

template <int BN> __device__ __forceinline__ int swzB(int k) {
  if constexpr (BN == 128) return ((k & 3) | ((k >> 1) & 4)) << 1;
  else return (((k >> 1) & 1) | ((k >> 2) & 2)) << 1;
}

template <int BM, int BN, bool SWAP, bool LNORM>
__device__ __forceinline__ void gemm_loop(const u16* __restrict__ A, int lda, const u16* __restrict__ B, int ldb,
                                          int K, char* lds, const float* stats,
                                          f32x4 (&acc)[BM / 32][BN / 32]) {
  constexpr int MT = BM / 32, NT = BN / 32;
  constexpr int NA = BM / 32;
  constexpr int CPR = BN / 8;
  constexpr int NB = CPR / 4;
  constexpr int ABYTES = BM * 128, BBYTES = 64 * BN * 2;
  const int tid = threadIdx.x, lane = tid & 63, wid = tid >> 6;
  const int wm = wid >> 1, wn = wid & 1;
  const int g = lane >> 4, li = lane & 15;
  char* ldsA = lds;
  char* ldsB = lds + 2 * ABYTES;

  float st_m[NA], st_r[NA];
  if constexpr (LNORM) {
#pragma unroll
    for (int i = 0; i < NA; ++i) {
      int row = (tid >> 3) + 32 * i;
      st_m[i] = stats[row * 2];
      st_r[i] = stats[row * 2 + 1];
    }
  }
  u32x4 ra[NA], rb[NB];
  auto gload = [&](int kt) {
#pragma unroll
    for (int i = 0; i < NA; ++i) {
      int row = (tid >> 3) + 32 * i, ch = tid & 7;
      ra[i] = *reinterpret_cast<const u32x4*>(A + (long)row * lda + kt * 64 + ch * 8);
    }
#pragma unroll
    for (int i = 0; i < NB; ++i) {
      int idx = tid + 256 * i, kr = idx / CPR, ch = idx % CPR;
      rb[i] = *reinterpret_cast<const u32x4*>(B + (long)(kt * 64 + kr) * ldb + ch * 8);
    }
  };
  auto lwrite = [&](int buf) {
#pragma unroll
    for (int i = 0; i < NA; ++i) {
      int row = (tid >> 3) + 32 * i, ch = tid & 7;
      u32x4 v = ra[i];
      if constexpr (LNORM) {
#pragma unroll
        for (int e = 0; e < 4; ++e) {
          float lo = __uint_as_float(v[e] << 16), hi = __uint_as_float(v[e] & 0xffff0000u);
          lo = rbf(lo - st_m[i]) / st_r[i];
          hi = rbf(hi - st_m[i]) / st_r[i];
          v[e] = pack2(lo, hi);
        }
      }
      *reinterpret_cast<u32x4*>(ldsA + buf * ABYTES + row * 128 + ((ch ^ ((row >> 1) & 7)) << 4)) = v;
    }
#pragma unroll
    for (int i = 0; i < NB; ++i) {
      int idx = tid + 256 * i, kr = idx / CPR, ch = idx % CPR;
      *reinterpret_cast<u32x4*>(ldsB + buf * BBYTES + kr * (BN * 2) + ((ch ^ swzB<BN>(kr)) << 4)) = rb[i];
    }
  };

  const int nk = K / 64;
  gload(0);
  lwrite(0);
  __syncthreads();
  for (int kt = 0; kt < nk; ++kt) {
    const int buf = kt & 1;
    if (kt + 1 < nk) gload(kt + 1);
    const char* cA = ldsA + buf * ABYTES;
    const char* cB = ldsB + buf * BBYTES;
#pragma unroll
    for (int kk = 0; kk < 2; ++kk) {
      bf16x8 af[MT], bfr[NT];
#pragma unroll
      for (int mt = 0; mt < MT; ++mt) {
        int row = wm * (BM / 2) + mt * 16 + li, ch = kk * 4 + g;
        af[mt] = *reinterpret_cast<const bf16x8*>(cA + row * 128 + ((ch ^ ((row >> 1) & 7)) << 4));
      }
#pragma unroll
      for (int nt = 0; nt < NT; ++nt) {
        int col = wn * (BN / 2) + nt * 16 + 4 * (li & 3);
        int ch = col >> 3, hb = (li & 1) * 8;
        int k0 = kk * 32 + 8 * g + (li >> 2), k1 = k0 + 4;
        s16x4 lo = __builtin_amdgcn_ds_read_tr16_b64_v4i16(
            (lds_s16x4*)(cB + k0 * (BN * 2) + ((ch ^ swzB<BN>(k0)) << 4) + hb));
        s16x4 hi = __builtin_amdgcn_ds_read_tr16_b64_v4i16(
            (lds_s16x4*)(cB + k1 * (BN * 2) + ((ch ^ swzB<BN>(k1)) << 4) + hb));
        bfr[nt] = bf16x8{lo[0], lo[1], lo[2], lo[3], hi[0], hi[1], hi[2], hi[3]};
      }
#pragma unroll
      for (int mt = 0; mt < MT; ++mt)
#pragma unroll
        for (int nt = 0; nt < NT; ++nt) {
          if constexpr (SWAP)
            acc[mt][nt] = __builtin_amdgcn_mfma_f32_16x16x32_bf16(bfr[nt], af[mt], acc[mt][nt], 0, 0, 0);
          else
            acc[mt][nt] = __builtin_amdgcn_mfma_f32_16x16x32_bf16(af[mt], bfr[nt], acc[mt][nt], 0, 0, 0);
        }
    }
    if (kt + 1 < nk) lwrite(buf ^ 1);
    __syncthreads();
  }
}

__global__ __launch_bounds__(256, 2) void k_gemm1(const u16* __restrict__ x, const u16* __restrict__ wqkv,
                                                  const u16* __restrict__ w1, u16* __restrict__ Qp,
                                                  u16* __restrict__ Kp, u16* __restrict__ Vt,
                                                  u16* __restrict__ Hb) {
  __shared__ __attribute__((aligned(16))) char lds[65536 + 1024];
  float* stats = reinterpret_cast<float*>(lds + 65536);
  const int bn = blockIdx.x % 27, bm = blockIdx.x / 27;
  const int tid = threadIdx.x, lane = tid & 63, wid = tid >> 6;
  const int g = lane >> 4, li = lane & 15;
  const u16* A = x + (long)bm * 128 * D_;

#pragma unroll 2
  for (int it = 0; it < 8; ++it) {
    int row = wid * 32 + it * 4 + g;
    float v[40];
#pragma unroll
    for (int c = 0; c < 5; ++c) {
      u32x4 q = *reinterpret_cast<const u32x4*>(A + (long)row * D_ + (c * 16 + li) * 8);
#pragma unroll
      for (int e = 0; e < 4; ++e) {
        v[c * 8 + 2 * e] = __uint_as_float(q[e] << 16);
        v[c * 8 + 2 * e + 1] = __uint_as_float(q[e] & 0xffff0000u);
      }
    }
    float s = 0.f;
#pragma unroll
    for (int e = 0; e < 40; ++e) s += v[e];
#pragma unroll
    for (int o = 1; o < 16; o <<= 1) s += __shfl_xor(s, o);
    float mean = s * (1.0f / D_);
    float q2 = 0.f;
#pragma unroll
    for (int e = 0; e < 40; ++e) { float d = v[e] - mean; q2 += d * d; }
#pragma unroll
    for (int o = 1; o < 16; o <<= 1) q2 += __shfl_xor(q2, o);
    if (li == 0) {
      float var = rbf(q2 * (1.0f / D_));
      stats[row * 2] = rbf(mean);
      stats[row * 2 + 1] = rbf(sqrtf(rbf(var + 1e-5f)));
    }
  }
  __syncthreads();

  const int wm = wid >> 1, wn = wid & 1;
  f32x4 acc[4][4];
#pragma unroll
  for (int i = 0; i < 4; ++i)
#pragma unroll
    for (int j = 0; j < 4; ++j) acc[i][j] = f32x4{0.f, 0.f, 0.f, 0.f};

  if (bn >= 10 && bn < 15) {
    gemm_loop<128, 128, false, true>(A, D_, wqkv + bn * 128, NQKV_, D_, lds, stats, acc);
#pragma unroll
    for (int mt = 0; mt < 4; ++mt)
#pragma unroll
      for (int nt = 0; nt < 4; ++nt) {
        int nn = bn * 128 - 1280 + wn * 64 + nt * 16 + li;
        int h = nn / DH_, d = nn % DH_;
        int m = bm * 128 + wm * 64 + mt * 16 + 4 * g;
        u32x2 o = {pack2(acc[mt][nt][0], acc[mt][nt][1]), pack2(acc[mt][nt][2], acc[mt][nt][3])};
        *reinterpret_cast<u32x2*>(Vt + ((long)(h * 32 + d)) * S_ + m) = o;
      }
  } else {
    const u16* Bp = (bn < 15) ? (wqkv + bn * 128) : (w1 + (bn - 15) * 128);
    const int ldb = (bn < 15) ? NQKV_ : FF_;
    gemm_loop<128, 128, true, true>(A, D_, Bp, ldb, D_, lds, stats, acc);
    if (bn < 10) {
      u16* P = (bn < 5) ? Qp : Kp;
      const int nb = (bn < 5) ? bn * 128 : (bn - 5) * 128;
#pragma unroll
      for (int mt = 0; mt < 4; ++mt)
#pragma unroll
        for (int nt = 0; nt < 4; ++nt) {
          int nn = nb + wn * 64 + nt * 16 + 4 * g;
          int h = nn / DH_, d = nn % DH_;
          int m = bm * 128 + wm * 64 + mt * 16 + li;
          u32x2 o = {pack2(acc[mt][nt][0], acc[mt][nt][1]), pack2(acc[mt][nt][2], acc[mt][nt][3])};
          u16* dst = P + ((long)h * S_ + m) * 32 + d;
          *reinterpret_cast<u32x2*>(dst) = o;
          if (d == 16) {
            u32x2 z = {0u, 0u};
            *reinterpret_cast<u32x2*>(dst + 4) = z;
            *reinterpret_cast<u32x2*>(dst + 8) = z;
            *reinterpret_cast<u32x2*>(dst + 12) = z;
          }
        }
    } else {
#pragma unroll
      for (int mt = 0; mt < 4; ++mt)
#pragma unroll
        for (int nt = 0; nt < 4; ++nt) {
          int nn = (bn - 15) * 128 + wn * 64 + nt * 16 + 4 * g;
          int m = bm * 128 + wm * 64 + mt * 16 + li;
          f32x4 a = acc[mt][nt];
          u32x2 o = {pack2(fmaxf(a[0], 0.f), fmaxf(a[1], 0.f)), pack2(fmaxf(a[2], 0.f), fmaxf(a[3], 0.f))};
          *reinterpret_cast<u32x2*>(Hb + (long)m * FF_ + nn) = o;
        }
    }
  }
}

__global__ __launch_bounds__(256) void k_attn(const u16* __restrict__ Qp, const u16* __restrict__ Kp,
                                              const u16* __restrict__ Vt, u16* __restrict__ O) {
  const int lane = threadIdx.x & 63, wid = threadIdx.x >> 6;
  const int g = lane >> 4, li = lane & 15;
  const int h = blockIdx.y;
  const int q0 = (blockIdx.x * 4 + wid) * 16;
  const u16* Qh = Qp + (long)h * S_ * 32;
  const u16* Kh = Kp + (long)h * S_ * 32;
  const u16* Vh = Vt + (long)h * 32 * S_;
  const float scale = 0.22360679774997896f;

  bf16x8 qf = *reinterpret_cast<const bf16x8*>(Qh + (long)(q0 + li) * 32 + g * 8);
  f32x4 s[17];
  const float NEG = -1e30f;
#pragma unroll
  for (int t = 0; t < 17; ++t) {
    int key0 = q0 - 256 + 16 * t;
    int kc = key0 < 0 ? 0 : key0;
    bf16x8 kf = *reinterpret_cast<const bf16x8*>(Kh + (long)(kc + li) * 32 + g * 8);
    f32x4 z = {0.f, 0.f, 0.f, 0.f};
    f32x4 r = __builtin_amdgcn_mfma_f32_16x16x32_bf16(kf, qf, z, 0, 0, 0);
#pragma unroll
    for (int e = 0; e < 4; ++e) {
      float v = rbf(rbf(r[e]) * scale);
      int o = 4 * g + e;
      bool ok = key0 >= 0;
      if (t == 0) ok = ok && (o >= li + 1);
      if (t == 16) ok = ok && (o <= li);
      r[e] = ok ? v : NEG;
    }
    s[t] = r;
  }
  float mx = NEG;
#pragma unroll
  for (int t = 0; t < 17; ++t)
#pragma unroll
    for (int e = 0; e < 4; ++e) mx = fmaxf(mx, s[t][e]);
  mx = fmaxf(mx, __shfl_xor(mx, 16));
  mx = fmaxf(mx, __shfl_xor(mx, 32));
  float sum = 0.f;
  const float L2E = 1.4426950408889634f;
#pragma unroll
  for (int t = 0; t < 17; ++t)
#pragma unroll
    for (int e = 0; e < 4; ++e) {
      float p = __builtin_amdgcn_exp2f((s[t][e] - mx) * L2E);
      p = rbf(p);
      s[t][e] = p;
      sum += p;
    }
  sum += __shfl_xor(sum, 16);
  sum += __shfl_xor(sum, 32);

  f32x4 o0 = {0.f, 0.f, 0.f, 0.f}, o1 = {0.f, 0.f, 0.f, 0.f};
  const int d1 = (16 + li) > 19 ? 19 : (16 + li);
#pragma unroll
  for (int u = 0; u < 9; ++u) {
    const int t0 = 2 * u, t1 = (u == 8) ? 16 : 2 * u + 1;
    int ka = q0 - 256 + 16 * t0, kb = q0 - 256 + 16 * t1;
    ka = ka < 0 ? 0 : ka;
    kb = kb < 0 ? 0 : kb;
    u32x4 pb;
    pb[0] = pack2(s[t0][0], s[t0][1]);
    pb[1] = pack2(s[t0][2], s[t0][3]);
    if (u == 8) { pb[2] = 0u; pb[3] = 0u; }
    else { pb[2] = pack2(s[t1][0], s[t1][1]); pb[3] = pack2(s[t1][2], s[t1][3]); }
    bf16x8 pf = __builtin_bit_cast(bf16x8, pb);
    u32x2 a0 = *reinterpret_cast<const u32x2*>(Vh + (long)li * S_ + ka + 4 * g);
    u32x2 a1 = *reinterpret_cast<const u32x2*>(Vh + (long)li * S_ + kb + 4 * g);
    u32x2 b0 = *reinterpret_cast<const u32x2*>(Vh + (long)d1 * S_ + ka + 4 * g);
    u32x2 b1 = *reinterpret_cast<const u32x2*>(Vh + (long)d1 * S_ + kb + 4 * g);
    u32x4 va = {a0[0], a0[1], a1[0], a1[1]}, vb = {b0[0], b0[1], b1[0], b1[1]};
    o0 = __builtin_amdgcn_mfma_f32_16x16x32_bf16(__builtin_bit_cast(bf16x8, va), pf, o0, 0, 0, 0);
    o1 = __builtin_amdgcn_mfma_f32_16x16x32_bf16(__builtin_bit_cast(bf16x8, vb), pf, o1, 0, 0, 0);
  }
  float inv = 1.0f / sum;
  u16* dst = O + (long)(q0 + li) * D_ + h * DH_;
  u32x2 w0 = {pack2(o0[0] * inv, o0[1] * inv), pack2(o0[2] * inv, o0[3] * inv)};
  *reinterpret_cast<u32x2*>(dst + 4 * g) = w0;
  if (g == 0) {
    u32x2 w1 = {pack2(o1[0] * inv, o1[1] * inv), pack2(o1[2] * inv, o1[3] * inv)};
    *reinterpret_cast<u32x2*>(dst + 16) = w1;
  }
}

__global__ __launch_bounds__(256) void k_gemm2(const u16* __restrict__ x, const u16* __restrict__ O,
                                               const u16* __restrict__ wo, const u16* __restrict__ Hb,
                                               const u16* __restrict__ w2, u16* __restrict__ out) {
  __shared__ __attribute__((aligned(16))) char lds[32768];
  const int bn = blockIdx.x % 10, bm = blockIdx.x / 10;
  const int tid = threadIdx.x, lane = tid & 63, wid = tid >> 6;
  const int g = lane >> 4, li = lane & 15, wm = wid >> 1, wn = wid & 1;
  f32x4 as[2][2], af[2][2];
#pragma unroll
  for (int i = 0; i < 2; ++i)
#pragma unroll
    for (int j = 0; j < 2; ++j) { as[i][j] = f32x4{0.f, 0.f, 0.f, 0.f}; af[i][j] = f32x4{0.f, 0.f, 0.f, 0.f}; }
  gemm_loop<64, 64, true, false>(O + (long)bm * 64 * D_, D_, wo + bn * 64, D_, D_, lds, nullptr, as);
  gemm_loop<64, 64, true, false>(Hb + (long)bm * 64 * FF_, FF_, w2 + bn * 64, D_, FF_, lds, nullptr, af);
#pragma unroll
  for (int mt = 0; mt < 2; ++mt)
#pragma unroll
    for (int nt = 0; nt < 2; ++nt) {
      int n = bn * 64 + wn * 32 + nt * 16 + 4 * g;
      int m = bm * 64 + wm * 32 + mt * 16 + li;
      u32x2 xv = *reinterpret_cast<const u32x2*>(x + (long)m * D_ + n);
      float xs[4] = {__uint_as_float(xv[0] << 16), __uint_as_float(xv[0] & 0xffff0000u),
                     __uint_as_float(xv[1] << 16), __uint_as_float(xv[1] & 0xffff0000u)};
      float r[4];
#pragma unroll
      for (int e = 0; e < 4; ++e) {
        float sv = rbf(as[mt][nt][e]);
        float f = rbf(af[mt][nt][e]);
        float gg = rbf(f / (1.0f + __expf(-f)));
        r[e] = xs[e] + rbf(sv * gg);
      }
      u32x2 o = {pack2(r[0], r[1]), pack2(r[2], r[3])};
      *reinterpret_cast<u32x2*>(out + (long)m * D_ + n) = o;
    }
}

extern "C" void kernel_launch(void* const* d_in, const int* in_sizes, int n_in, void* d_out, int out_size,
                              void* d_ws, size_t ws_size, hipStream_t stream) {
  const u16* x = (const u16*)d_in[0];
  const u16* wqkv = (const u16*)d_in[1];
  const u16* wo = (const u16*)d_in[2];
  const u16* w1 = (const u16*)d_in[4];
  const u16* w2 = (const u16*)d_in[5];
  u16* out = (u16*)d_out;
  char* ws = (char*)d_ws;
  u16* Qp = (u16*)(ws);
  u16* Kp = (u16*)(ws + (4l << 20));
  u16* Vt = (u16*)(ws + (8l << 20));
  u16* Hb = (u16*)(ws + (12l << 20));
  u16* O = (u16*)(ws + (20l << 20));
  k_gemm1<<<16 * 27, 256, 0, stream>>>(x, wqkv, w1, Qp, Kp, Vt, Hb);
  k_attn<<<dim3(32, 32), 256, 0, stream>>>(Qp, Kp, Vt, O);
  k_gemm2<<<32 * 10, 256, 0, stream>>>(x, O, wo, Hb, w2, out);
}
```

```cpp
#include <hip/hip_runtime.h>
#include <hip/hip_bf16.h>


typedef unsigned short u16;
using bf16x8 = __attribute__((ext_vector_type(8))) short;
using s16x4  = __attribute__((ext_vector_type(4))) short;
using f32x4  = __attribute__((ext_vector_type(4))) float;
using u32x4  = __attribute__((ext_vector_type(4))) unsigned;
using u32x2  = __attribute__((ext_vector_type(2))) unsigned;
typedef __attribute__((address_space(3))) s16x4 lds_s16x4;

constexpr int S_ = 2048, D_ = 640, H_ = 32, DH_ = 20, FF_ = 1536, NQKV_ = 1920;

__device__ __forceinline__ float bf2f(u16 v) { return __uint_as_float(((unsigned)v) << 16); }
__device__ __forceinline__ u16 f2bf(float f) {
  __bf16 b = (__bf16)f;
  return __builtin_bit_cast(u16, b);
}
__device__ __forceinline__ float rbf(float f) { return bf2f(f2bf(f)); }
__device__ __forceinline__ unsigned pack2(float a, float b) {
  return (unsigned)f2bf(a) | ((unsigned)f2bf(b) << 16);
}

template <int BN> __device__ __forceinline__ int swzB(int k) {
  if constexpr (BN == 128) return ((k & 3) | ((k >> 1) & 4)) << 1;
  else return (((k >> 1) & 1) | ((k >> 2) & 2)) << 1;
}

template <int N> __device__ __forceinline__ void wait_vm() { asm volatile("s_waitcnt vmcnt(%0)" ::"i"(N) : "memory"); }

template <int BM, int BN, int NS, bool SWAP>
__device__ __forceinline__ void gemm_loop(const u16* __restrict__ A, int lda, const u16* __restrict__ B, int ldb,
                                          int nk, char* lds, f32x4 (&acc)[BM / 32][BN / 32]) {
  constexpr int MT = BM / 32, NT = BN / 32;
  constexpr int NA = BM / 32, NB = BN / 32, PER = NA + NB;
  constexpr int ABYTES = BM * 128, BBYTES = 64 * BN * 2, SBYTES = ABYTES + BBYTES;
  const int tid = threadIdx.x, lane = tid & 63;
  const int wid = __builtin_amdgcn_readfirstlane(tid >> 6);
  const int wm = wid >> 1, wn = wid & 1;
  const int g = lane >> 4, li = lane & 15;

  const u16* pa[NA];
  const u16* pb[NB];
#pragma unroll
  for (int j = 0; j < NA; ++j) {
    int row = (wid * NA + j) * 8 + (lane >> 3);
    int lc = (lane & 7) ^ ((row >> 1) & 7);
    pa[j] = A + (long)row * lda + lc * 8;
  }
#pragma unroll
  for (int j = 0; j < NB; ++j) {
    constexpr int CPR = BN / 8, RPI = 64 / CPR;
    int kr = (wid * NB + j) * RPI + lane / CPR;
    int lc = (lane % CPR) ^ swzB<BN>(kr);
    pb[j] = B + (long)kr * ldb + lc * 8;
  }
  auto issue = [&](int kt) {
    char* sb = lds + (kt % NS) * SBYTES;
#pragma unroll
    for (int j = 0; j < NA; ++j)
      __builtin_amdgcn_global_load_lds((const unsigned*)(pa[j] + kt * 64),
                                       (unsigned*)(sb + (wid * NA + j) * 1024 + lane * 16), 16, 0, 0);
#pragma unroll
    for (int j = 0; j < NB; ++j)
      __builtin_amdgcn_global_load_lds((const unsigned*)(pb[j] + (long)kt * 64 * ldb),
                                       (unsigned*)(sb + ABYTES + (wid * NB + j) * 1024 + lane * 16), 16, 0, 0);
  };
#pragma unroll
  for (int s = 0; s < NS - 1; ++s) issue(s);
  for (int kt = 0; kt < nk; ++kt) {
    int rem = nk - 1 - kt;
    if (rem >= NS - 2) wait_vm<(NS - 2) * PER>();
    else if (NS >= 4 && rem == 1) wait_vm<PER>();
    else wait_vm<0>();
    __builtin_amdgcn_s_barrier();
    asm volatile("" ::: "memory");
    if (kt + NS - 1 < nk) issue(kt + NS - 1);
    const char* cA = lds + (kt % NS) * SBYTES;
    const char* cB = cA + ABYTES;
#pragma unroll
    for (int kk = 0; kk < 2; ++kk) {
      bf16x8 af[MT], bfr[NT];
#pragma unroll
      for (int mt = 0; mt < MT; ++mt) {
        int row = wm * (BM / 2) + mt * 16 + li, ch = kk * 4 + g;
        af[mt] = *reinterpret_cast<const bf16x8*>(cA + row * 128 + ((ch ^ ((row >> 1) & 7)) << 4));
      }
#pragma unroll
      for (int nt = 0; nt < NT; ++nt) {
        int col = wn * (BN / 2) + nt * 16 + 4 * (li & 3);
        int ch = col >> 3, hb = (li & 1) * 8;
        int k0 = kk * 32 + 8 * g + (li >> 2), k1 = k0 + 4;
        s16x4 lo = __builtin_amdgcn_ds_read_tr16_b64_v4i16(
            (lds_s16x4*)(cB + k0 * (BN * 2) + ((ch ^ swzB<BN>(k0)) << 4) + hb));
        s16x4 hi = __builtin_amdgcn_ds_read_tr16_b64_v4i16(
            (lds_s16x4*)(cB + k1 * (BN * 2) + ((ch ^ swzB<BN>(k1)) << 4) + hb));
        bfr[nt] = bf16x8{lo[0], lo[1], lo[2], lo[3], hi[0], hi[1], hi[2], hi[3]};
      }
#pragma unroll
      for (int mt = 0; mt < MT; ++mt)
#pragma unroll
        for (int nt = 0; nt < NT; ++nt) {
          if constexpr (SWAP)
            acc[mt][nt] = __builtin_amdgcn_mfma_f32_16x16x32_bf16(bfr[nt], af[mt], acc[mt][nt], 0, 0, 0);
          else
            acc[mt][nt] = __builtin_amdgcn_mfma_f32_16x16x32_bf16(af[mt], bfr[nt], acc[mt][nt], 0, 0, 0);
        }
    }
  }
  __builtin_amdgcn_s_barrier();
  asm volatile("" ::: "memory");
}

__global__ __launch_bounds__(256) void k_ln(const u16* __restrict__ x, u16* __restrict__ xn) {
  const int lane = threadIdx.x & 63, li = lane & 15;
  const int row = blockIdx.x * 16 + (threadIdx.x >> 4);
  float v[40];
#pragma unroll
  for (int c = 0; c < 5; ++c) {
    u32x4 q = *reinterpret_cast<const u32x4*>(x + (long)row * D_ + (c * 16 + li) * 8);
#pragma unroll
    for (int e = 0; e < 4; ++e) {
      v[c * 8 + 2 * e] = __uint_as_float(q[e] << 16);
      v[c * 8 + 2 * e + 1] = __uint_as_float(q[e] & 0xffff0000u);
    }
  }
  float s = 0.f;
#pragma unroll
  for (int e = 0; e < 40; ++e) s += v[e];
#pragma unroll
  for (int o = 1; o < 16; o <<= 1) s += __shfl_xor(s, o);
  float mean = s * (1.0f / D_);
  float q2 = 0.f;
#pragma unroll
  for (int e = 0; e < 40; ++e) { float d = v[e] - mean; q2 += d * d; }
#pragma unroll
  for (int o = 1; o < 16; o <<= 1) q2 += __shfl_xor(q2, o);
  float var = rbf(q2 * (1.0f / D_));
  float mb = rbf(mean);
  float den = rbf(sqrtf(rbf(var + 1e-5f)));
#pragma unroll
  for (int c = 0; c < 5; ++c) {
    u32x4 o;
#pragma unroll
    for (int e = 0; e < 4; ++e)
      o[e] = pack2(rbf(v[c * 8 + 2 * e] - mb) / den, rbf(v[c * 8 + 2 * e + 1] - mb) / den);
    *reinterpret_cast<u32x4*>(xn + (long)row * D_ + (c * 16 + li) * 8) = o;
  }
}

constexpr int NS1 = 3;
__global__ __launch_bounds__(256) void k_gemm1(const u16* __restrict__ xn, const u16* __restrict__ wqkv,
                                               const u16* __restrict__ w1, u16* __restrict__ Qp,
                                               u16* __restrict__ Kp, u16* __restrict__ Vt,
                                               u16* __restrict__ Hb) {
  __shared__ __attribute__((aligned(16))) char lds[NS1 * 32768];
  const int bn = blockIdx.x % 27, bm = blockIdx.x / 27;
  const int tid = threadIdx.x, lane = tid & 63, wid = tid >> 6;
  const int g = lane >> 4, li = lane & 15;
  const u16* A = xn + (long)bm * 128 * D_;
  const int wm = wid >> 1, wn = wid & 1;
  f32x4 acc[4][4];
#pragma unroll
  for (int i = 0; i < 4; ++i)
#pragma unroll
    for (int j = 0; j < 4; ++j) acc[i][j] = f32x4{0.f, 0.f, 0.f, 0.f};

  if (bn >= 10 && bn < 15) {
    gemm_loop<128, 128, NS1, false>(A, D_, wqkv + bn * 128, NQKV_, D_ / 64, lds, acc);
#pragma unroll
    for (int mt = 0; mt < 4; ++mt)
#pragma unroll
      for (int nt = 0; nt < 4; ++nt) {
        int nn = bn * 128 - 1280 + wn * 64 + nt * 16 + li;
        int h = nn / DH_, d = nn % DH_;
        int m = bm * 128 + wm * 64 + mt * 16;
        u32x2 o = {pack2(acc[mt][nt][0], acc[mt][nt][1]), pack2(acc[mt][nt][2], acc[mt][nt][3])};
        long off = ((((long)h * 64 + (m >> 5)) * 32 + d) * 4 + g) * 8 + ((m >> 4) & 1) * 4;
        *reinterpret_cast<u32x2*>(Vt + off) = o;
      }
  } else {
    const u16* Bp = (bn < 15) ? (wqkv + bn * 128) : (w1 + (bn - 15) * 128);
    const int ldb = (bn < 15) ? NQKV_ : FF_;
    gemm_loop<128, 128, NS1, true>(A, D_, Bp, ldb, D_ / 64, lds, acc);
    if (bn < 10) {
      u16* P = (bn < 5) ? Qp : Kp;
      const int nb = (bn < 5) ? bn * 128 : (bn - 5) * 128;
#pragma unroll
      for (int mt = 0; mt < 4; ++mt)
#pragma unroll
        for (int nt = 0; nt < 4; ++nt) {
          int nn = nb + wn * 64 + nt * 16 + 4 * g;
          int h = nn / DH_, d = nn % DH_;
          int m = bm * 128 + wm * 64 + mt * 16 + li;
          u32x2 o = {pack2(acc[mt][nt][0], acc[mt][nt][1]), pack2(acc[mt][nt][2], acc[mt][nt][3])};
          u16* dst = P + ((long)h * S_ + m) * 32 + d;
          *reinterpret_cast<u32x2*>(dst) = o;
          if (d == 16) {
            u32x2 z = {0u, 0u};
            *reinterpret_cast<u32x2*>(dst + 4) = z;
            *reinterpret_cast<u32x2*>(dst + 8) = z;
            *reinterpret_cast<u32x2*>(dst + 12) = z;
          }
        }
    } else {
#pragma unroll
      for (int mt = 0; mt < 4; ++mt)
#pragma unroll
        for (int nt = 0; nt < 4; ++nt) {
          int nn = (bn - 15) * 128 + wn * 64 + nt * 16 + 4 * g;
          int m = bm * 128 + wm * 64 + mt * 16 + li;
          f32x4 a = acc[mt][nt];
          u32x2 o = {pack2(fmaxf(a[0], 0.f), fmaxf(a[1], 0.f)), pack2(fmaxf(a[2], 0.f), fmaxf(a[3], 0.f))};
          *reinterpret_cast<u32x2*>(Hb + (long)m * FF_ + nn) = o;
        }
    }
  }
}

__global__ __launch_bounds__(256) void k_attn(const u16* __restrict__ Qp, const u16* __restrict__ Kp,
                                              const u16* __restrict__ Vt, u16* __restrict__ O) {
  const int lane = threadIdx.x & 63;
  const int wid = __builtin_amdgcn_readfirstlane(threadIdx.x >> 6);
  const int g = lane >> 4, li = lane & 15;
  const int h = blockIdx.y;
  const int q0 = (blockIdx.x * 4 + wid) * 16;
  const int base = (q0 - 256) & ~31;
  const u16* Qh = Qp + (long)h * S_ * 32;
  const u16* Kh = Kp + (long)h * S_ * 32;
  const u16* Vh = Vt + (long)h * 64 * 1024;
  const float C = 0.22360679774997896f * 1.4426950408889634f;
  const float NEG = -3e38f;
  const int q = q0 + li;

  bf16x8 qf = *reinterpret_cast<const bf16x8*>(Qh + (long)q * 32 + g * 8);
  f32x4 s[18];
#pragma unroll
  for (int t = 0; t < 18; ++t) {
    int key0 = base + 16 * t;
    int kc = key0 < 0 ? 0 : key0;
    bf16x8 kf = *reinterpret_cast<const bf16x8*>(Kh + (long)(kc + li) * 32 + g * 8);
    f32x4 z = {0.f, 0.f, 0.f, 0.f};
    s[t] = __builtin_amdgcn_mfma_f32_16x16x32_bf16(kf, qf, z, 0, 0, 0);
  }
#pragma unroll
  for (int t = 0; t < 18; ++t) {
    int key0 = base + 16 * t;
    if (t < 2 || t >= 16) {
#pragma unroll
      for (int e = 0; e < 4; ++e) {
        int key = key0 + 4 * g + e;
        bool ok = (key >= 0) && (key <= q) && (q - key < 256);
        s[t][e] = ok ? s[t][e] : NEG;
      }
    } else if (key0 < 0) {
      s[t] = f32x4{NEG, NEG, NEG, NEG};
    }
  }
  float mx = NEG;
#pragma unroll
  for (int t = 0; t < 18; ++t)
#pragma unroll
    for (int e = 0; e < 4; ++e) mx = fmaxf(mx, s[t][e]);
  mx = fmaxf(mx, __shfl_xor(mx, 16));
  mx = fmaxf(mx, __shfl_xor(mx, 32));
  const float mc = -mx * C;
  float sum = 0.f;
  f32x4 o0 = {0.f, 0.f, 0.f, 0.f}, o1 = {0.f, 0.f, 0.f, 0.f};
  const int d1 = (16 + li) > 19 ? 19 : (16 + li);
#pragma unroll
  for (int u = 0; u < 9; ++u) {
    int kp = base + 32 * u;
    kp = kp < 0 ? 0 : kp;
    const u16* vp = Vh + (long)(kp >> 5) * 1024 + g * 8;
    bf16x8 va = *reinterpret_cast<const bf16x8*>(vp + li * 32);
    bf16x8 vb = *reinterpret_cast<const bf16x8*>(vp + d1 * 32);
    float p[8];
#pragma unroll
    for (int e = 0; e < 4; ++e) {
      p[e] = __builtin_amdgcn_exp2f(fmaf(s[2 * u][e], C, mc));
      p[4 + e] = __builtin_amdgcn_exp2f(fmaf(s[2 * u + 1][e], C, mc));
    }
#pragma unroll
    for (int e = 0; e < 8; ++e) sum += p[e];
    u32x4 pb = {pack2(p[0], p[1]), pack2(p[2], p[3]), pack2(p[4], p[5]), pack2(p[6], p[7])};
    bf16x8 pf = __builtin_bit_cast(bf16x8, pb);
    o0 = __builtin_amdgcn_mfma_f32_16x16x32_bf16(va, pf, o0, 0, 0, 0);
    o1 = __builtin_amdgcn_mfma_f32_16x16x32_bf16(vb, pf, o1, 0, 0, 0);
  }
  sum += __shfl_xor(sum, 16);
  sum += __shfl_xor(sum, 32);
  float inv = 1.0f / sum;
  u16* dst = O + (long)q * D_ + h * DH_;
  u32x2 w0 = {pack2(o0[0] * inv, o0[1] * inv), pack2(o0[2] * inv, o0[3] * inv)};
  *reinterpret_cast<u32x2*>(dst + 4 * g) = w0;
  if (g == 0) {
    u32x2 w1 = {pack2(o1[0] * inv, o1[1] * inv), pack2(o1[2] * inv, o1[3] * inv)};
    *reinterpret_cast<u32x2*>(dst + 16) = w1;
  }
}

constexpr int NS2 = 4;
__global__ __launch_bounds__(256) void k_gemm2(const u16* __restrict__ x, const u16* __restrict__ O,
                                               const u16* __restrict__ wo, const u16* __restrict__ Hb,
                                               const u16* __restrict__ w2, u16* __restrict__ out) {
  __shared__ __attribute__((aligned(16))) char lds[NS2 * 16384];
  const int bn = blockIdx.x % 10, bm = blockIdx.x / 10;
  const int tid = threadIdx.x, lane = tid & 63, wid = tid >> 6;
  const int g = lane >> 4, li = lane & 15, wm = wid >> 1, wn = wid & 1;
  f32x4 as[2][2], af[2][2];
#pragma unroll
  for (int i = 0; i < 2; ++i)
#pragma unroll
    for (int j = 0; j < 2; ++j) { as[i][j] = f32x4{0.f, 0.f, 0.f, 0.f}; af[i][j] = f32x4{0.f, 0.f, 0.f, 0.f}; }
  gemm_loop<64, 64, NS2, true>(Hb + (long)bm * 64 * FF_, FF_, w2 + bn * 64, D_, FF_ / 64, lds, af);
  gemm_loop<64, 64, NS2, true>(O + (long)bm * 64 * D_, D_, wo + bn * 64, D_, D_ / 64, lds, as);
#pragma unroll
  for (int mt = 0; mt < 2; ++mt)
#pragma unroll
    for (int nt = 0; nt < 2; ++nt) {
      int n = bn * 64 + wn * 32 + nt * 16 + 4 * g;
      int m = bm * 64 + wm * 32 + mt * 16 + li;
      u32x2 xv = *reinterpret_cast<const u32x2*>(x + (long)m * D_ + n);
      float xs[4] = {__uint_as_float(xv[0] << 16), __uint_as_float(xv[0] & 0xffff0000u),
                     __uint_as_float(xv[1] << 16), __uint_as_float(xv[1] & 0xffff0000u)};
      float r[4];
#pragma unroll
      for (int e = 0; e < 4; ++e) {
        float sv = rbf(as[mt][nt][e]);
        float f = rbf(af[mt][nt][e]);
        float gg = rbf(f / (1.0f + __expf(-f)));
        r[e] = xs[e] + rbf(sv * gg);
      }
      u32x2 o = {pack2(r[0], r[1]), pack2(r[2], r[3])};
      *reinterpret_cast<u32x2*>(out + (long)m * D_ + n) = o;
    }
}

extern "C" void kernel_launch(void* const* d_in, const int* in_sizes, int n_in, void* d_out, int out_size,
                              void* d_ws, size_t ws_size, hipStream_t stream) {
  const u16* x = (const u16*)d_in[0];
  const u16* wqkv = (const u16*)d_in[1];
  const u16* wo = (const u16*)d_in[2];
  const u16* w1 = (const u16*)d_in[4];
  const u16* w2 = (const u16*)d_in[5];
  u16* out = (u16*)d_out;
  char* ws = (char*)d_ws;
  u16* Qp = (u16*)(ws);
  u16* Kp = (u16*)(ws + (4l << 20));
  u16* Vt = (u16*)(ws + (8l << 20));
  u16* Hb = (u16*)(ws + (12l << 20));
  u16* O = (u16*)(ws + (20l << 20));
  u16* xn = (u16*)(ws + (24l << 20));
  k_ln<<<128, 256, 0, stream>>>(x, xn);
  k_gemm1<<<16 * 27, 256, 0, stream>>>(xn, wqkv, w1, Qp, Kp, Vt, Hb);
  k_attn<<<dim3(32, 32), 256, 0, stream>>>(Qp, Kp, Vt, O);
  k_gemm2<<<32 * 10, 256, 0, stream>>>(x, O, wo, Hb, w2, out);
}
```

```cpp
#include <hip/hip_runtime.h>
#include <hip/hip_bf16.h>


typedef unsigned short u16;
using bf16x8 = __attribute__((ext_vector_type(8))) short;
using s16x4  = __attribute__((ext_vector_type(4))) short;
using f32x4  = __attribute__((ext_vector_type(4))) float;
using u32x4  = __attribute__((ext_vector_type(4))) unsigned;
using u32x2  = __attribute__((ext_vector_type(2))) unsigned;
typedef __attribute__((address_space(3))) s16x4 lds_s16x4;

constexpr int S_ = 2048, D_ = 640, H_ = 32, DH_ = 20, FF_ = 1536, NQKV_ = 1920;

__device__ __forceinline__ float bf2f(u16 v) { return __uint_as_float(((unsigned)v) << 16); }
__device__ __forceinline__ u16 f2bf(float f) {
  __bf16 b = (__bf16)f;
  return __builtin_bit_cast(u16, b);
}
__device__ __forceinline__ float rbf(float f) { return bf2f(f2bf(f)); }
__device__ __forceinline__ unsigned pack2(float a, float b) {
  return (unsigned)f2bf(a) | ((unsigned)f2bf(b) << 16);
}

template <int BN> __device__ __forceinline__ int swzB(int k) {
  if constexpr (BN == 128) return ((k & 3) | ((k >> 1) & 4)) << 1;
  else return (((k >> 1) & 1) | ((k >> 2) & 2)) << 1;
}

template <int N> __device__ __forceinline__ void wait_vm() { asm volatile("s_waitcnt vmcnt(%0)" ::"i"(N) : "memory"); }

template <int BM, int BN, int WGM, int WGN, int NS>
__device__ __forceinline__ void gemm_loop(const u16* __restrict__ A, int lda, const u16* __restrict__ B, int ldb,
                                          int nk, char* lds, f32x4 (&acc)[BM / WGM / 16][BN / WGN / 16]) {
  constexpr int NW = WGM * WGN;
  constexpr int MT = BM / WGM / 16, NT = BN / WGN / 16;
  constexpr int NA = BM / 8 / NW, NB = BN / 8 / NW, PER = NA + NB;
  constexpr int ABYTES = BM * 128, BBYTES = 64 * BN * 2, SBYTES = ABYTES + BBYTES;
  const int tid = threadIdx.x, lane = tid & 63;
  const int wid = __builtin_amdgcn_readfirstlane(tid >> 6);
  const int wm = wid / WGN, wn = wid % WGN;
  const int g = lane >> 4, li = lane & 15;

  const u16* pa[NA];
  const u16* pb[NB];
#pragma unroll
  for (int j = 0; j < NA; ++j) {
    int row = (wid * NA + j) * 8 + (lane >> 3);
    int lc = (lane & 7) ^ ((row >> 1) & 7);
    pa[j] = A + (long)row * lda + lc * 8;
  }
#pragma unroll
  for (int j = 0; j < NB; ++j) {
    constexpr int CPR = BN / 8, RPI = 64 / CPR;
    int kr = (wid * NB + j) * RPI + lane / CPR;
    int lc = (lane % CPR) ^ swzB<BN>(kr);
    pb[j] = B + (long)kr * ldb + lc * 8;
  }
  auto issue = [&](int kt) {
    char* sb = lds + (kt % NS) * SBYTES;
#pragma unroll
    for (int j = 0; j < NA; ++j)
      __builtin_amdgcn_global_load_lds((const unsigned*)(pa[j] + kt * 64),
                                       (unsigned*)(sb + (wid * NA + j) * 1024 + lane * 16), 16, 0, 0);
#pragma unroll
    for (int j = 0; j < NB; ++j)
      __builtin_amdgcn_global_load_lds((const unsigned*)(pb[j] + (long)kt * 64 * ldb),
                                       (unsigned*)(sb + ABYTES + (wid * NB + j) * 1024 + lane * 16), 16, 0, 0);
  };
#pragma unroll
  for (int s = 0; s < NS - 1; ++s) issue(s);
  for (int kt = 0; kt < nk; ++kt) {
    int rem = nk - 1 - kt;
    if (rem >= NS - 2) wait_vm<(NS - 2) * PER>();
    else if (NS >= 5 && rem == 2) wait_vm<2 * PER>();
    else if (NS >= 4 && rem == 1) wait_vm<PER>();
    else wait_vm<0>();
    __builtin_amdgcn_s_barrier();
    asm volatile("" ::: "memory");
    if (kt + NS - 1 < nk) issue(kt + NS - 1);
    const char* cA = lds + (kt % NS) * SBYTES;
    const char* cB = cA + ABYTES;
#pragma unroll
    for (int kk = 0; kk < 2; ++kk) {
      bf16x8 af[MT], bfr[NT];
#pragma unroll
      for (int mt = 0; mt < MT; ++mt) {
        int row = wm * (MT * 16) + mt * 16 + li, ch = kk * 4 + g;
        af[mt] = *reinterpret_cast<const bf16x8*>(cA + row * 128 + ((ch ^ ((row >> 1) & 7)) << 4));
      }
#pragma unroll
      for (int nt = 0; nt < NT; ++nt) {
        int col = wn * (NT * 16) + nt * 16 + 4 * (li & 3);
        int ch = col >> 3, hb = (li & 1) * 8;
        int k0 = kk * 32 + 8 * g + (li >> 2), k1 = k0 + 4;
        s16x4 lo = __builtin_amdgcn_ds_read_tr16_b64_v4i16(
            (lds_s16x4*)(cB + k0 * (BN * 2) + ((ch ^ swzB<BN>(k0)) << 4) + hb));
        s16x4 hi = __builtin_amdgcn_ds_read_tr16_b64_v4i16(
            (lds_s16x4*)(cB + k1 * (BN * 2) + ((ch ^ swzB<BN>(k1)) << 4) + hb));
        bfr[nt] = bf16x8{lo[0], lo[1], lo[2], lo[3], hi[0], hi[1], hi[2], hi[3]};
      }
#pragma unroll
      for (int mt = 0; mt < MT; ++mt)
#pragma unroll
        for (int nt = 0; nt < NT; ++nt)
          acc[mt][nt] = __builtin_amdgcn_mfma_f32_16x16x32_bf16(bfr[nt], af[mt], acc[mt][nt], 0, 0, 0);
    }
  }
  __builtin_amdgcn_s_barrier();
  asm volatile("" ::: "memory");
}

__global__ __launch_bounds__(256) void k_ln(const u16* __restrict__ x, u16* __restrict__ xn) {

  const int lane = threadIdx.x & 63, li = lane & 15;
  const int row = blockIdx.x * 16 + (threadIdx.x >> 4);
  float v[40];
#pragma unroll
  for (int c = 0; c < 5; ++c) {
    u32x4 q = *reinterpret_cast<const u32x4*>(x + (long)row * D_ + (c * 16 + li) * 8);
#pragma unroll
    for (int e = 0; e < 4; ++e) {
      v[c * 8 + 2 * e] = __uint_as_float(q[e] << 16);
      v[c * 8 + 2 * e + 1] = __uint_as_float(q[e] & 0xffff0000u);
    }
  }
  float s = 0.f;
#pragma unroll
  for (int e = 0; e < 40; ++e) s += v[e];
#pragma unroll
  for (int o = 1; o < 16; o <<= 1) s += __shfl_xor(s, o);
  float mean = s * (1.0f / D_);
  float q2 = 0.f;
#pragma unroll
  for (int e = 0; e < 40; ++e) { float d = v[e] - mean; q2 += d * d; }
#pragma unroll
  for (int o = 1; o < 16; o <<= 1) q2 += __shfl_xor(q2, o);
  float var = rbf(q2 * (1.0f / D_));
  float mb = rbf(mean);
  float den = rbf(sqrtf(rbf(var + 1e-5f)));
#pragma unroll
  for (int c = 0; c < 5; ++c) {
    u32x4 o;
#pragma unroll
    for (int e = 0; e < 4; ++e)
      o[e] = pack2(rbf(v[c * 8 + 2 * e] - mb) / den, rbf(v[c * 8 + 2 * e + 1] - mb) / den);
    *reinterpret_cast<u32x4*>(xn + (long)row * D_ + (c * 16 + li) * 8) = o;
  }
}

constexpr int NS1 = 3;
__global__ __launch_bounds__(512) void k_gemm1(const u16* __restrict__ xn, const u16* __restrict__ wqkv,
                                               const u16* __restrict__ w1, u16* __restrict__ QKV,
                                               u16* __restrict__ Hb) {
  __shared__ __attribute__((aligned(16))) char lds[NS1 * 49152];
  const int tile = (blockIdx.x & 7) * 27 + (blockIdx.x >> 3);
  const int bn = tile >> 3, bm = tile & 7;
  const int tid = threadIdx.x, lane = tid & 63;
  const int wid = __builtin_amdgcn_readfirstlane(tid >> 6);
  const int g = lane >> 4, li = lane & 15;
  const int wm = wid >> 1, wn = wid & 1;
  const u16* A = xn + (long)bm * 256 * D_;
  const bool isH = bn >= 15;
  const u16* Bp = isH ? (w1 + (bn - 15) * 128) : (wqkv + bn * 128);
  const int ldb = isH ? FF_ : NQKV_;
  f32x4 acc[4][4];
#pragma unroll
  for (int i = 0; i < 4; ++i)
#pragma unroll
    for (int j = 0; j < 4; ++j) acc[i][j] = f32x4{0.f, 0.f, 0.f, 0.f};
  gemm_loop<256, 128, 4, 2, NS1>(A, D_, Bp, ldb, D_ / 64, lds, acc);

  char* wl = lds + wid * (64 * 144);
  const float lo = isH ? 0.f : -3.0e38f;
#pragma unroll
  for (int mt = 0; mt < 4; ++mt)
#pragma unroll
    for (int nt = 0; nt < 4; ++nt) {
      f32x4 a = acc[mt][nt];
      u32x2 o = {pack2(fmaxf(a[0], lo), fmaxf(a[1], lo)), pack2(fmaxf(a[2], lo), fmaxf(a[3], lo))};
      *reinterpret_cast<u32x2*>(wl + (mt * 16 + li) * 144 + (nt * 16 + 4 * g) * 2) = o;
    }
  u16* C = isH ? (Hb + (bn - 15) * 128) : (QKV + bn * 128);
  C += (long)(bm * 256 + wm * 64) * ldb + wn * 64;
#pragma unroll
  for (int it = 0; it < 8; ++it) {
    int r = it * 8 + (lane >> 3), c = lane & 7;
    u32x4 v = *reinterpret_cast<const u32x4*>(wl + r * 144 + c * 16);
    *reinterpret_cast<u32x4*>(C + (long)r * ldb + c * 8) = v;
  }
}

__device__ __forceinline__ int kvsw(int row) { return (0x78 >> (2 * ((row >> 2) & 3))) & 3; }

__global__ __launch_bounds__(256) void k_attn(const u16* __restrict__ QKV, u16* __restrict__ O) {
  __shared__ __attribute__((aligned(16))) char lds[2 * 384 * 64];
  char* Ks = lds;
  char* Vs = lds + 384 * 64;
  const int tid = threadIdx.x, lane = tid & 63;
  const int wid = __builtin_amdgcn_readfirstlane(tid >> 6);
  const int g = lane >> 4, li = lane & 15;
  const int loc = blockIdx.x >> 3;
  const int h = (blockIdx.x & 7) * 4 + (loc >> 4);
  const int qblk = loc & 15;
  const int kbase = qblk * 128 - 256;

  {
    u32x2 kv[12], vv[12];
#pragma unroll
    for (int i = 0; i < 12; ++i) {
      int idx = tid + 256 * i, kr = idx >> 3, sl = idx & 7;
      int key = kbase + kr;
      u32x2 z = {0u, 0u};
      kv[i] = z; vv[i] = z;
      if (sl < 5 && key >= 0) {
        const u16* src = QKV + (long)key * NQKV_ + 640 + h * DH_ + sl * 4;
        kv[i] = *reinterpret_cast<const u32x2*>(src);
        vv[i] = *reinterpret_cast<const u32x2*>(src + 640);
      }
    }
#pragma unroll
    for (int i = 0; i < 12; ++i) {
      int idx = tid + 256 * i, kr = idx >> 3, sl = idx & 7;
      int off = kr * 64 + (((sl >> 1) ^ kvsw(kr)) << 4) + (sl & 1) * 8;
      *reinterpret_cast<u32x2*>(Ks + off) = kv[i];
      *reinterpret_cast<u32x2*>(Vs + off) = vv[i];
    }
  }
  const int q0 = qblk * 128 + wid * 32;
  bf16x8 qf[2];
#pragma unroll
  for (int a = 0; a < 2; ++a) {
    const u16* src = QKV + (long)(q0 + a * 16 + li) * NQKV_ + h * DH_ + g * 8;
    u32x2 x0 = {0u, 0u}, x1 = {0u, 0u};
    if (g < 3) x0 = *reinterpret_cast<const u32x2*>(src);
    if (g < 2) x1 = *reinterpret_cast<const u32x2*>(src + 4);
    u32x4 t = {x0[0], x0[1], x1[0], x1[1]};
    qf[a] = __builtin_bit_cast(bf16x8, t);
  }
  __syncthreads();

  const float C = 0.22360679774997896f * 1.4426950408889634f;
  const float NEG = -3e38f;
  const int lb = wid * 32;
  const int base = q0 - 256;
  f32x4 s[2][18];
#pragma unroll
  for (int t = 0; t < 18; ++t) {
    int r = lb + 16 * t + li;
    bf16x8 kf = *reinterpret_cast<const bf16x8*>(Ks + r * 64 + ((g ^ kvsw(r)) << 4));
    f32x4 z = {0.f, 0.f, 0.f, 0.f};
    s[0][t] = __builtin_amdgcn_mfma_f32_16x16x32_bf16(kf, qf[0], z, 0, 0, 0);
    s[1][t] = __builtin_amdgcn_mfma_f32_16x16x32_bf16(kf, qf[1], z, 0, 0, 0);
  }
  float mc[2];
#pragma unroll
  for (int a = 0; a < 2; ++a) {
    const int q = q0 + a * 16 + li;
#pragma unroll
    for (int t = 0; t < 18; ++t) {
      int key0 = base + 16 * t;
      if (t < 2 || t >= 16) {
#pragma unroll
        for (int e = 0; e < 4; ++e) {
          int key = key0 + 4 * g + e;
          bool ok = (key >= 0) && (key <= q) && (q - key < 256);
          s[a][t][e] = ok ? s[a][t][e] : NEG;
        }
      } else if (key0 < 0) {
        s[a][t] = f32x4{NEG, NEG, NEG, NEG};
      }
    }
    float mx = NEG;
#pragma unroll
    for (int t = 0; t < 18; ++t)
#pragma unroll
      for (int e = 0; e < 4; ++e) mx = fmaxf(mx, s[a][t][e]);
    mx = fmaxf(mx, __shfl_xor(mx, 16));
    mx = fmaxf(mx, __shfl_xor(mx, 32));
    mc[a] = -mx * C;
  }
  float sum[2] = {0.f, 0.f};
  f32x4 o[2][2];
#pragma unroll
  for (int a = 0; a < 2; ++a) { o[a][0] = f32x4{0.f, 0.f, 0.f, 0.f}; o[a][1] = f32x4{0.f, 0.f, 0.f, 0.f}; }
#pragma unroll
  for (int u = 0; u < 9; ++u) {
    bf16x8 vf[2];
#pragma unroll
    for (int dt = 0; dt < 2; ++dt) {
      int r0 = lb + 32 * u + 4 * g + (li >> 2), r1 = r0 + 16;
      int ch = dt * 2 + ((li & 3) >> 1), hb = (li & 1) * 8;
      s16x4 lo = __builtin_amdgcn_ds_read_tr16_b64_v4i16((lds_s16x4*)(Vs + r0 * 64 + ((ch ^ kvsw(r0)) << 4) + hb));
      s16x4 hi = __builtin_amdgcn_ds_read_tr16_b64_v4i16((lds_s16x4*)(Vs + r1 * 64 + ((ch ^ kvsw(r1)) << 4) + hb));
      vf[dt] = bf16x8{lo[0], lo[1], lo[2], lo[3], hi[0], hi[1], hi[2], hi[3]};
    }
#pragma unroll
    for (int a = 0; a < 2; ++a) {
      float p[8];
#pragma unroll
      for (int e = 0; e < 4; ++e) {
        p[e] = __builtin_amdgcn_exp2f(fmaf(s[a][2 * u][e], C, mc[a]));
        p[4 + e] = __builtin_amdgcn_exp2f(fmaf(s[a][2 * u + 1][e], C, mc[a]));
      }
#pragma unroll
      for (int e = 0; e < 8; ++e) sum[a] += p[e];
      u32x4 pb = {pack2(p[0], p[1]), pack2(p[2], p[3]), pack2(p[4], p[5]), pack2(p[6], p[7])};
      bf16x8 pf = __builtin_bit_cast(bf16x8, pb);
      o[a][0] = __builtin_amdgcn_mfma_f32_16x16x32_bf16(vf[0], pf, o[a][0], 0, 0, 0);
      o[a][1] = __builtin_amdgcn_mfma_f32_16x16x32_bf16(vf[1], pf, o[a][1], 0, 0, 0);
    }
  }
#pragma unroll
  for (int a = 0; a < 2; ++a) {
    float sm = sum[a];
    sm += __shfl_xor(sm, 16);
    sm += __shfl_xor(sm, 32);
    float inv = 1.0f / sm;
    u16* dst = O + (long)(q0 + a * 16 + li) * D_ + h * DH_;
    u32x2 w0 = {pack2(o[a][0][0] * inv, o[a][0][1] * inv), pack2(o[a][0][2] * inv, o[a][0][3] * inv)};
    *reinterpret_cast<u32x2*>(dst + 4 * g) = w0;
    if (g == 0) {
      u32x2 w1 = {pack2(o[a][1][0] * inv, o[a][1][1] * inv), pack2(o[a][1][2] * inv, o[a][1][3] * inv)};
      *reinterpret_cast<u32x2*>(dst + 16) = w1;
    }
  }
}

constexpr int NS2 = 4;
__global__ __launch_bounds__(256) void k_gemm2(const u16* __restrict__ x, const u16* __restrict__ O,
                                               const u16* __restrict__ wo, const u16* __restrict__ Hb,
                                               const u16* __restrict__ w2, u16* __restrict__ out) {
  __shared__ __attribute__((aligned(16))) char lds[NS2 * 16384];

  const int loc = blockIdx.x >> 3;
  const int bm = (blockIdx.x & 7) * 4 + (loc & 3), bn = loc >> 2;
  const int tid = threadIdx.x, lane = tid & 63, wid = tid >> 6;
  const int g = lane >> 4, li = lane & 15, wm = wid >> 1, wn = wid & 1;
  f32x4 as[2][2], af[2][2];
#pragma unroll
  for (int i = 0; i < 2; ++i)
#pragma unroll
    for (int j = 0; j < 2; ++j) { as[i][j] = f32x4{0.f, 0.f, 0.f, 0.f}; af[i][j] = f32x4{0.f, 0.f, 0.f, 0.f}; }
  gemm_loop<64, 64, 2, 2, NS2>(Hb + (long)bm * 64 * FF_, FF_, w2 + bn * 64, D_, FF_ / 64, lds, af);
  gemm_loop<64, 64, 2, 2, NS2>(O + (long)bm * 64 * D_, D_, wo + bn * 64, D_, D_ / 64, lds, as);
#pragma unroll
  for (int mt = 0; mt < 2; ++mt)
#pragma unroll
    for (int nt = 0; nt < 2; ++nt) {
      int n = bn * 64 + wn * 32 + nt * 16 + 4 * g;
      int m = bm * 64 + wm * 32 + mt * 16 + li;
      u32x2 xv = *reinterpret_cast<const u32x2*>(x + (long)m * D_ + n);
      float xs[4] = {__uint_as_float(xv[0] << 16), __uint_as_float(xv[0] & 0xffff0000u),
                     __uint_as_float(xv[1] << 16), __uint_as_float(xv[1] & 0xffff0000u)};
      float r[4];
#pragma unroll
      for (int e = 0; e < 4; ++e) {
        float sv = rbf(as[mt][nt][e]);
        float f = rbf(af[mt][nt][e]);
        float gg = rbf(f / (1.0f + __expf(-f)));
        r[e] = xs[e] + rbf(sv * gg);
      }
      u32x2 o = {pack2(r[0], r[1]), pack2(r[2], r[3])};
      *reinterpret_cast<u32x2*>(out + (long)m * D_ + n) = o;
    }
}

extern "C" void kernel_launch(void* const* d_in, const int* in_sizes, int n_in, void* d_out, int out_size,
                              void* d_ws, size_t ws_size, hipStream_t stream) {
  const u16* x = (const u16*)d_in[0];
  const u16* wqkv = (const u16*)d_in[1];
  const u16* wo = (const u16*)d_in[2];
  const u16* w1 = (const u16*)d_in[4];
  const u16* w2 = (const u16*)d_in[5];
  u16* out = (u16*)d_out;
  char* ws = (char*)d_ws;
  u16* QKV = (u16*)(ws);
  u16* Hb = (u16*)(ws + (8l << 20));
  u16* O = (u16*)(ws + (16l << 20));
  u16* xn = (u16*)(ws + (20l << 20));
  k_ln<<<128, 256, 0, stream>>>(x, xn);
  k_gemm1<<<8 * 27, 512, 0, stream>>>(xn, wqkv, w1, QKV, Hb);
  k_attn<<<512, 256, 0, stream>>>(QKV, O);
  k_gemm2<<<32 * 10, 256, 0, stream>>>(x, O, wo, Hb, w2, out);
}
```

```cpp
#include <hip/hip_runtime.h>
#include <hip/hip_bf16.h>


typedef unsigned short u16;
using bf16x8 = __attribute__((ext_vector_type(8))) short;
using s16x4  = __attribute__((ext_vector_type(4))) short;
using f32x4  = __attribute__((ext_vector_type(4))) float;
using u32x4  = __attribute__((ext_vector_type(4))) unsigned;
using u32x2  = __attribute__((ext_vector_type(2))) unsigned;
typedef __attribute__((address_space(3))) s16x4 lds_s16x4;

constexpr int S_ = 2048, D_ = 640, H_ = 32, DH_ = 20, FF_ = 1536, NQKV_ = 1920;

__device__ __forceinline__ float bf2f(u16 v) { return __uint_as_float(((unsigned)v) << 16); }
__device__ __forceinline__ u16 f2bf(float f) {
  __bf16 b = (__bf16)f;
  return __builtin_bit_cast(u16, b);
}
__device__ __forceinline__ float rbf(float f) { return bf2f(f2bf(f)); }
typedef __attribute__((ext_vector_type(2))) float f32x2;
typedef __attribute__((ext_vector_type(2))) __bf16 bf16x2_t;
__device__ __forceinline__ unsigned pack2(float a, float b) {
  f32x2 v = {a, b};
  return __builtin_bit_cast(unsigned, __builtin_convertvector(v, bf16x2_t));
}

template <int BN> __device__ __forceinline__ int swzB(int k) {
  if constexpr (BN == 128) return ((k & 3) | ((k >> 1) & 4)) << 1;
  else return (((k >> 1) & 1) | ((k >> 2) & 2)) << 1;
}

template <int N> __device__ __forceinline__ void wait_vm() { asm volatile("s_waitcnt vmcnt(%0)" ::"i"(N) : "memory"); }

template <int BM, int BN, int WGM, int WGN, int NS>
__device__ __forceinline__ void gemm_loop(const u16* __restrict__ A, int lda, const u16* __restrict__ B, int ldb,
                                          int nk, char* lds, f32x4 (&acc)[BM / WGM / 16][BN / WGN / 16]) {
  constexpr int NW = WGM * WGN;
  constexpr int MT = BM / WGM / 16, NT = BN / WGN / 16;
  constexpr int NA = BM / 8 / NW, NB = BN / 8 / NW, PER = NA + NB;
  constexpr int ABYTES = BM * 128, BBYTES = 64 * BN * 2, SBYTES = ABYTES + BBYTES;
  const int tid = threadIdx.x, lane = tid & 63;
  const int wid = __builtin_amdgcn_readfirstlane(tid >> 6);
  const int wm = wid / WGN, wn = wid % WGN;
  const int g = lane >> 4, li = lane & 15;

  const u16* pa[NA];
  const u16* pb[NB];
#pragma unroll
  for (int j = 0; j < NA; ++j) {
    int row = (wid * NA + j) * 8 + (lane >> 3);
    int lc = (lane & 7) ^ ((row >> 1) & 7);
    pa[j] = A + (long)row * lda + lc * 8;
  }
#pragma unroll
  for (int j = 0; j < NB; ++j) {
    constexpr int CPR = BN / 8, RPI = 64 / CPR;
    int kr = (wid * NB + j) * RPI + lane / CPR;
    int lc = (lane % CPR) ^ swzB<BN>(kr);
    pb[j] = B + (long)kr * ldb + lc * 8;
  }
  auto issue = [&](int kt) {
    char* sb = lds + (kt % NS) * SBYTES;
#pragma unroll
    for (int j = 0; j < NA; ++j)
      __builtin_amdgcn_global_load_lds((const unsigned*)(pa[j] + kt * 64),
                                       (unsigned*)(sb + (wid * NA + j) * 1024 + lane * 16), 16, 0, 0);
#pragma unroll
    for (int j = 0; j < NB; ++j)
      __builtin_amdgcn_global_load_lds((const unsigned*)(pb[j] + (long)kt * 64 * ldb),
                                       (unsigned*)(sb + ABYTES + (wid * NB + j) * 1024 + lane * 16), 16, 0, 0);
  };
  static_assert(NS == 3, "ring of 3");
  auto rdfrag = [&](int kt, int kk, bf16x8 (&af)[MT], bf16x8 (&bfr)[NT]) {
    const char* cA = lds + (kt % NS) * SBYTES;
    const char* cB = cA + ABYTES;
#pragma unroll
    for (int mt = 0; mt < MT; ++mt) {
      int row = wm * (MT * 16) + mt * 16 + li, ch = kk * 4 + g;
      af[mt] = *reinterpret_cast<const bf16x8*>(cA + row * 128 + ((ch ^ ((row >> 1) & 7)) << 4));
    }
#pragma unroll
    for (int nt = 0; nt < NT; ++nt) {
      int col = wn * (NT * 16) + nt * 16 + 4 * (li & 3);
      int ch = col >> 3, hb = (li & 1) * 8;
      int k0 = kk * 32 + 8 * g + (li >> 2), k1 = k0 + 4;
      s16x4 lo = __builtin_amdgcn_ds_read_tr16_b64_v4i16(
          (lds_s16x4*)(cB + k0 * (BN * 2) + ((ch ^ swzB<BN>(k0)) << 4) + hb));
      s16x4 hi = __builtin_amdgcn_ds_read_tr16_b64_v4i16(
          (lds_s16x4*)(cB + k1 * (BN * 2) + ((ch ^ swzB<BN>(k1)) << 4) + hb));
      bfr[nt] = bf16x8{lo[0], lo[1], lo[2], lo[3], hi[0], hi[1], hi[2], hi[3]};
    }
  };
  auto mma = [&](bf16x8 (&af)[MT], bf16x8 (&bfr)[NT], int m0, int m1) {
#pragma unroll
    for (int mt = m0; mt < m1; ++mt)
#pragma unroll
      for (int nt = 0; nt < NT; ++nt)
        acc[mt][nt] = __builtin_amdgcn_mfma_f32_16x16x32_bf16(bfr[nt], af[mt], acc[mt][nt], 0, 0, 0);
  };
  bf16x8 a0[MT], b0[NT], a1[MT], b1[NT];
  issue(0); issue(1); issue(2);
  wait_vm<2 * PER>();
  __builtin_amdgcn_s_barrier();
  asm volatile("" ::: "memory");
  rdfrag(0, 0, a0, b0);
  for (int kt = 0; kt < nk; ++kt) {
    mma(a0, b0, 0, 1);
    __builtin_amdgcn_sched_barrier(0);
    rdfrag(kt, 1, a1, b1);
    __builtin_amdgcn_sched_barrier(0);
    mma(a0, b0, 1, MT);
    __builtin_amdgcn_sched_barrier(0);
    if (kt + 1 < nk) {
      __builtin_amdgcn_s_waitcnt(0xC07F);
      if (kt + 2 < nk) wait_vm<PER>(); else wait_vm<0>();
      __builtin_amdgcn_s_barrier();
      asm volatile("" ::: "memory");
      if (kt + 3 < nk) issue(kt + 3);
      rdfrag(kt + 1, 0, a0, b0);
    }
    __builtin_amdgcn_sched_barrier(0);
    mma(a1, b1, 0, MT);
    __builtin_amdgcn_sched_barrier(0);
  }
  __builtin_amdgcn_s_barrier();
  asm volatile("" ::: "memory");
}

__global__ __launch_bounds__(256) void k_ln(const u16* __restrict__ x, u16* __restrict__ xn) {

  const int lane = threadIdx.x & 63, li = lane & 15;
  const int row = blockIdx.x * 16 + (threadIdx.x >> 4);
  float v[40];
#pragma unroll
  for (int c = 0; c < 5; ++c) {
    u32x4 q = *reinterpret_cast<const u32x4*>(x + (long)row * D_ + (c * 16 + li) * 8);
#pragma unroll
    for (int e = 0; e < 4; ++e) {
      v[c * 8 + 2 * e] = __uint_as_float(q[e] << 16);
      v[c * 8 + 2 * e + 1] = __uint_as_float(q[e] & 0xffff0000u);
    }
  }
  float s = 0.f;
#pragma unroll
  for (int e = 0; e < 40; ++e) s += v[e];
#pragma unroll
  for (int o = 1; o < 16; o <<= 1) s += __shfl_xor(s, o);
  float mean = s * (1.0f / D_);
  float q2 = 0.f;
#pragma unroll
  for (int e = 0; e < 40; ++e) { float d = v[e] - mean; q2 += d * d; }
#pragma unroll
  for (int o = 1; o < 16; o <<= 1) q2 += __shfl_xor(q2, o);
  float var = rbf(q2 * (1.0f / D_));
  float mb = rbf(mean);
  float den = rbf(sqrtf(rbf(var + 1e-5f)));
  float rden = 1.0f / den;
#pragma unroll
  for (int c = 0; c < 5; ++c) {
    u32x4 o;
#pragma unroll
    for (int e = 0; e < 4; ++e)
      o[e] = pack2(rbf(v[c * 8 + 2 * e] - mb) * rden, rbf(v[c * 8 + 2 * e + 1] - mb) * rden);
    *reinterpret_cast<u32x4*>(xn + (long)row * D_ + (c * 16 + li) * 8) = o;
  }
}

constexpr int NS1 = 3;
__global__ __launch_bounds__(512) void k_gemm1(const u16* __restrict__ xn, const u16* __restrict__ wqkv,
                                               const u16* __restrict__ w1, u16* __restrict__ QKV,
                                               u16* __restrict__ Hb) {
  __shared__ __attribute__((aligned(16))) char lds[NS1 * 49152];
  const int tile = (blockIdx.x & 7) * 27 + (blockIdx.x >> 3);
  const int bn = tile >> 3, bm = tile & 7;
  const int tid = threadIdx.x, lane = tid & 63;
  const int wid = __builtin_amdgcn_readfirstlane(tid >> 6);
  const int g = lane >> 4, li = lane & 15;
  const int wm = wid >> 1, wn = wid & 1;
  const u16* A = xn + (long)bm * 256 * D_;
  const bool isH = bn >= 15;
  const u16* Bp = isH ? (w1 + (bn - 15) * 128) : (wqkv + bn * 128);
  const int ldb = isH ? FF_ : NQKV_;
  f32x4 acc[4][4];
#pragma unroll
  for (int i = 0; i < 4; ++i)
#pragma unroll
    for (int j = 0; j < 4; ++j) acc[i][j] = f32x4{0.f, 0.f, 0.f, 0.f};
  gemm_loop<256, 128, 4, 2, NS1>(A, D_, Bp, ldb, D_ / 64, lds, acc);

  char* wl = lds + wid * (64 * 144);
  const float lo = isH ? 0.f : -3.0e38f;
#pragma unroll
  for (int mt = 0; mt < 4; ++mt)
#pragma unroll
    for (int nt = 0; nt < 4; ++nt) {
      f32x4 a = acc[mt][nt];
      u32x2 o = {pack2(fmaxf(a[0], lo), fmaxf(a[1], lo)), pack2(fmaxf(a[2], lo), fmaxf(a[3], lo))};
      *reinterpret_cast<u32x2*>(wl + (mt * 16 + li) * 144 + (nt * 16 + 4 * g) * 2) = o;
    }
  u16* C = isH ? (Hb + (bn - 15) * 128) : (QKV + bn * 128);
  C += (long)(bm * 256 + wm * 64) * ldb + wn * 64;
#pragma unroll
  for (int it = 0; it < 8; ++it) {
    int r = it * 8 + (lane >> 3), c = lane & 7;
    u32x4 v = *reinterpret_cast<const u32x4*>(wl + r * 144 + c * 16);
    *reinterpret_cast<u32x4*>(C + (long)r * ldb + c * 8) = v;
  }
}

__device__ __forceinline__ int kvsw(int row) { return (0x78 >> (2 * ((row >> 2) & 3))) & 3; }

__device__ __forceinline__ void attn_block(const u16* __restrict__ QKV, u16* __restrict__ O, char* lds, int h, int qblk) {
  char* Ks = lds;
  char* Vs = lds + 384 * 64;
  const int tid = threadIdx.x, lane = tid & 63;
  const int wid = __builtin_amdgcn_readfirstlane(tid >> 6);
  const int g = lane >> 4, li = lane & 15;
  const int kbase = qblk * 128 - 256;

  {
    u32x2 kv[12], vv[12];
#pragma unroll
    for (int i = 0; i < 12; ++i) {
      int idx = tid + 256 * i, kr = idx >> 3, sl = idx & 7;
      int key = kbase + kr;
      const bool ok = (sl < 5) && (key >= 0);
      const u16* src = QKV + (long)(key < 0 ? 0 : key) * NQKV_ + 640 + h * DH_ + (sl < 5 ? sl : 0) * 4;
      u32x2 a = *reinterpret_cast<const u32x2*>(src);
      u32x2 b = *reinterpret_cast<const u32x2*>(src + 640);
      kv[i] = u32x2{ok ? a[0] : 0u, ok ? a[1] : 0u};
      vv[i] = u32x2{ok ? b[0] : 0u, ok ? b[1] : 0u};
    }
#pragma unroll
    for (int i = 0; i < 12; ++i) {
      int idx = tid + 256 * i, kr = idx >> 3, sl = idx & 7;
      int off = kr * 64 + (((sl >> 1) ^ kvsw(kr)) << 4) + (sl & 1) * 8;
      *reinterpret_cast<u32x2*>(Ks + off) = kv[i];
      *reinterpret_cast<u32x2*>(Vs + off) = vv[i];
    }
  }
  const int q0 = qblk * 128 + wid * 32;
  bf16x8 qf[2];
#pragma unroll
  for (int a = 0; a < 2; ++a) {
    const u16* src = QKV + (long)(q0 + a * 16 + li) * NQKV_ + h * DH_;
    u32x2 x0 = *reinterpret_cast<const u32x2*>(src + (g < 3 ? g * 8 : 0));
    u32x2 x1 = *reinterpret_cast<const u32x2*>(src + (g < 2 ? g * 8 + 4 : 0));
    u32x4 t = {g < 3 ? x0[0] : 0u, g < 3 ? x0[1] : 0u, g < 2 ? x1[0] : 0u, g < 2 ? x1[1] : 0u};
    qf[a] = __builtin_bit_cast(bf16x8, t);
  }
  __syncthreads();

  const float C = 0.22360679774997896f * 1.4426950408889634f;
  const float NEG = -3e38f;
  const int lb = wid * 32;
  const int base = q0 - 256;
#pragma unroll 1
  for (int a = 0; a < 2; ++a) {
    const bf16x8 qa = a ? qf[1] : qf[0];
    const int q = q0 + a * 16 + li;
    f32x4 s[18];
#pragma unroll
    for (int t = 0; t < 18; ++t) {
      int r = lb + 16 * t + li;
      bf16x8 kf = *reinterpret_cast<const bf16x8*>(Ks + r * 64 + ((g ^ kvsw(r)) << 4));
      f32x4 z = {0.f, 0.f, 0.f, 0.f};
      s[t] = __builtin_amdgcn_mfma_f32_16x16x32_bf16(kf, qa, z, 0, 0, 0);
    }
#pragma unroll
    for (int t = 0; t < 18; ++t) {
      int key0 = base + 16 * t;
      if (t < 2 || t >= 16) {
#pragma unroll
        for (int e = 0; e < 4; ++e) {
          int key = key0 + 4 * g + e;
          bool ok = (key >= 0) && (key <= q) && (q - key < 256);
          s[t][e] = ok ? s[t][e] : NEG;
        }
      } else if (key0 < 0) {
        s[t] = f32x4{NEG, NEG, NEG, NEG};
      }
    }
    float mx = NEG;
#pragma unroll
    for (int t = 0; t < 18; ++t)
#pragma unroll
      for (int e = 0; e < 4; ++e) mx = fmaxf(mx, s[t][e]);
    mx = fmaxf(mx, __shfl_xor(mx, 16));
    mx = fmaxf(mx, __shfl_xor(mx, 32));
    const float mc = -mx * C;
    float sum = 0.f;
    f32x4 o0 = {0.f, 0.f, 0.f, 0.f}, o1 = {0.f, 0.f, 0.f, 0.f};
#pragma unroll
    for (int u = 0; u < 9; ++u) {
      bf16x8 vf[2];
#pragma unroll
      for (int dt = 0; dt < 2; ++dt) {
        int r0 = lb + 32 * u + 4 * g + (li >> 2), r1 = r0 + 16;
        int ch = dt * 2 + ((li & 3) >> 1), hb = (li & 1) * 8;
        s16x4 lo = __builtin_amdgcn_ds_read_tr16_b64_v4i16((lds_s16x4*)(Vs + r0 * 64 + ((ch ^ kvsw(r0)) << 4) + hb));
        s16x4 hi = __builtin_amdgcn_ds_read_tr16_b64_v4i16((lds_s16x4*)(Vs + r1 * 64 + ((ch ^ kvsw(r1)) << 4) + hb));
        vf[dt] = bf16x8{lo[0], lo[1], lo[2], lo[3], hi[0], hi[1], hi[2], hi[3]};
      }
      float p[8];
#pragma unroll
      for (int e = 0; e < 4; ++e) {
        p[e] = __builtin_amdgcn_exp2f(fmaf(s[2 * u][e], C, mc));
        p[4 + e] = __builtin_amdgcn_exp2f(fmaf(s[2 * u + 1][e], C, mc));
      }
#pragma unroll
      for (int e = 0; e < 8; ++e) sum += p[e];
      u32x4 pb = {pack2(p[0], p[1]), pack2(p[2], p[3]), pack2(p[4], p[5]), pack2(p[6], p[7])};
      bf16x8 pf = __builtin_bit_cast(bf16x8, pb);
      o0 = __builtin_amdgcn_mfma_f32_16x16x32_bf16(vf[0], pf, o0, 0, 0, 0);
      o1 = __builtin_amdgcn_mfma_f32_16x16x32_bf16(vf[1], pf, o1, 0, 0, 0);
    }
    sum += __shfl_xor(sum, 16);
    sum += __shfl_xor(sum, 32);
    float inv = 1.0f / sum;
    u16* dst = O + (long)q * D_ + h * DH_;
    u32x2 w0 = {pack2(o0[0] * inv, o0[1] * inv), pack2(o0[2] * inv, o0[3] * inv)};
    *reinterpret_cast<u32x2*>(dst + 4 * g) = w0;
    if (g == 0) {
      u32x2 w1 = {pack2(o1[0] * inv, o1[1] * inv), pack2(o1[2] * inv, o1[3] * inv)};
      *reinterpret_cast<u32x2*>(dst + 16) = w1;
    }
  }
}

__global__ __launch_bounds__(256, 2) void k_mid(const u16* __restrict__ QKV, u16* __restrict__ O,
                                                const u16* __restrict__ Hb, const u16* __restrict__ w2,
                                                float* __restrict__ Fp) {
  __shared__ __attribute__((aligned(16))) char lds[3 * 24576];
  const int xcd = blockIdx.x & 7, loc = blockIdx.x >> 3;
  const bool isAttn = (loc >= 120) || ((loc & 1) == 0);
  if (isAttn) {
    const int idx = loc >= 120 ? loc - 60 : (loc >> 1);
    attn_block(QKV, O, lds, xcd * 4 + (idx >> 4), idx & 15);
    return;
  }
  const int j = loc >> 1;
  const int bm = xcd * 2 + (j & 1);
  const int r = j >> 1;
  const int bn = r % 10, sp = r / 10;
  const int tid = threadIdx.x, lane = tid & 63;
  const int wid = __builtin_amdgcn_readfirstlane(tid >> 6);
  const int g = lane >> 4, li = lane & 15, wm = wid >> 1, wn = wid & 1;
  f32x4 acc[4][2];
#pragma unroll
  for (int i = 0; i < 4; ++i) { acc[i][0] = f32x4{0.f, 0.f, 0.f, 0.f}; acc[i][1] = f32x4{0.f, 0.f, 0.f, 0.f}; }
  gemm_loop<128, 64, 2, 2, 3>(Hb + (long)bm * 128 * FF_ + sp * 512, FF_, w2 + (long)sp * 512 * D_ + bn * 64, D_, 8,
                              lds, acc);
  float* dst = Fp + (long)sp * S_ * D_;
#pragma unroll
  for (int mt = 0; mt < 4; ++mt)
#pragma unroll
    for (int nt = 0; nt < 2; ++nt) {
      int m = bm * 128 + wm * 64 + mt * 16 + li;
      int n = bn * 64 + wn * 32 + nt * 16 + 4 * g;
      *reinterpret_cast<f32x4*>(dst + (long)m * D_ + n) = acc[mt][nt];
    }
}

__global__ __launch_bounds__(256) void k_gemm2(const u16* __restrict__ x, const u16* __restrict__ O,
                                               const u16* __restrict__ wo, const float* __restrict__ Fp,
                                               u16* __restrict__ out) {
  __shared__ __attribute__((aligned(16))) char lds[3 * 16384];
  const int loc = blockIdx.x >> 3;
  const int bm = (blockIdx.x & 7) * 4 + (loc & 3), bn = loc >> 2;
  const int tid = threadIdx.x, lane = tid & 63, wid = tid >> 6;
  const int g = lane >> 4, li = lane & 15, wm = wid >> 1, wn = wid & 1;
  u32x2 xv[2][2];
  f32x4 fv[2][2];
#pragma unroll
  for (int mt = 0; mt < 2; ++mt)
#pragma unroll
    for (int nt = 0; nt < 2; ++nt) {
      int n = bn * 64 + wn * 32 + nt * 16 + 4 * g;
      int m = bm * 64 + wm * 32 + mt * 16 + li;
      long off = (long)m * D_ + n;
      xv[mt][nt] = *reinterpret_cast<const u32x2*>(x + off);
      f32x4 f0 = *reinterpret_cast<const f32x4*>(Fp + off);
      f32x4 f1 = *reinterpret_cast<const f32x4*>(Fp + (long)S_ * D_ + off);
      f32x4 f2 = *reinterpret_cast<const f32x4*>(Fp + 2l * S_ * D_ + off);
      fv[mt][nt] = (f0 + f1) + f2;
    }
  f32x4 as[2][2];
#pragma unroll
  for (int i = 0; i < 2; ++i)
#pragma unroll
    for (int j = 0; j < 2; ++j) as[i][j] = f32x4{0.f, 0.f, 0.f, 0.f};
  gemm_loop<64, 64, 2, 2, 3>(O + (long)bm * 64 * D_, D_, wo + bn * 64, D_, D_ / 64, lds, as);
#pragma unroll
  for (int mt = 0; mt < 2; ++mt)
#pragma unroll
    for (int nt = 0; nt < 2; ++nt) {
      int n = bn * 64 + wn * 32 + nt * 16 + 4 * g;
      int m = bm * 64 + wm * 32 + mt * 16 + li;
      u32x2 xq = xv[mt][nt];
      float xs[4] = {__uint_as_float(xq[0] << 16), __uint_as_float(xq[0] & 0xffff0000u),
                     __uint_as_float(xq[1] << 16), __uint_as_float(xq[1] & 0xffff0000u)};
      float r[4];
#pragma unroll
      for (int e = 0; e < 4; ++e) {
        float sv = rbf(as[mt][nt][e]);
        float f = rbf(fv[mt][nt][e]);
        float gg = rbf(f / (1.0f + __expf(-f)));
        r[e] = xs[e] + rbf(sv * gg);
      }
      u32x2 o = {pack2(r[0], r[1]), pack2(r[2], r[3])};
      *reinterpret_cast<u32x2*>(out + (long)m * D_ + n) = o;
    }
}

extern "C" void kernel_launch(void* const* d_in, const int* in_sizes, int n_in, void* d_out, int out_size,
                              void* d_ws, size_t ws_size, hipStream_t stream) {
  const u16* x = (const u16*)d_in[0];
  const u16* wqkv = (const u16*)d_in[1];
  const u16* wo = (const u16*)d_in[2];
  const u16* w1 = (const u16*)d_in[4];
  const u16* w2 = (const u16*)d_in[5];
  u16* out = (u16*)d_out;
  char* ws = (char*)d_ws;
  u16* QKV = (u16*)(ws);
  u16* Hb = (u16*)(ws + (8l << 20));
  u16* O = (u16*)(ws + (16l << 20));
  u16* xn = (u16*)(ws + (20l << 20));
  k_ln<<<128, 256, 0, stream>>>(x, xn);
  k_gemm1<<<8 * 27, 512, 0, stream>>>(xn, wqkv, w1, QKV, Hb);
  float* Fp = (float*)(ws + (24l << 20));
  k_mid<<<8 * 124, 256, 0, stream>>>(QKV, O, Hb, w2, Fp);
  k_gemm2<<<32 * 10, 256, 0, stream>>>(x, O, wo, Fp, out);
}
```

```cpp
#include <hip/hip_runtime.h>
#include <hip/hip_bf16.h>


typedef unsigned short u16;
using bf16x8 = __attribute__((ext_vector_type(8))) short;
using s16x4  = __attribute__((ext_vector_type(4))) short;
using f32x4  = __attribute__((ext_vector_type(4))) float;
using u32x4  = __attribute__((ext_vector_type(4))) unsigned;
using u32x2  = __attribute__((ext_vector_type(2))) unsigned;
typedef __attribute__((address_space(3))) s16x4 lds_s16x4;

constexpr int S_ = 2048, D_ = 640, H_ = 32, DH_ = 20, FF_ = 1536, NQKV_ = 1920;

__device__ __forceinline__ float bf2f(u16 v) { return __uint_as_float(((unsigned)v) << 16); }
__device__ __forceinline__ u16 f2bf(float f) {
  __bf16 b = (__bf16)f;
  return __builtin_bit_cast(u16, b);
}
__device__ __forceinline__ float rbf(float f) { return bf2f(f2bf(f)); }
typedef __attribute__((ext_vector_type(2))) float f32x2;
typedef __attribute__((ext_vector_type(2))) __bf16 bf16x2_t;
__device__ __forceinline__ unsigned pack2(float a, float b) {
  f32x2 v = {a, b};
  return __builtin_bit_cast(unsigned, __builtin_convertvector(v, bf16x2_t));
}

template <int BN> __device__ __forceinline__ int swzB(int k) {
  if constexpr (BN == 128) return ((k & 3) | ((k >> 1) & 4)) << 1;
  else return (((k >> 1) & 1) | ((k >> 2) & 2)) << 1;
}

template <int N> __device__ __forceinline__ void wait_vm() { asm volatile("s_waitcnt vmcnt(%0)" ::"i"(N) : "memory"); }

__device__ __forceinline__ void acc_stats(const bf16x8& f, float& s1, float& s2) {
  u32x4 u = __builtin_bit_cast(u32x4, f);
#pragma unroll
  for (int e = 0; e < 4; ++e) {
    float lo = __uint_as_float(u[e] << 16), hi = __uint_as_float(u[e] & 0xffff0000u);
    s1 += lo; s2 = fmaf(lo, lo, s2);
    s1 += hi; s2 = fmaf(hi, hi, s2);
  }
}
__device__ __forceinline__ void acc_sum(const bf16x8& f, float& s1) {
  u32x4 u = __builtin_bit_cast(u32x4, f);
#pragma unroll
  for (int e = 0; e < 4; ++e) s1 += __uint_as_float(u[e] << 16) + __uint_as_float(u[e] & 0xffff0000u);
}

template <int BM, int BN, int WGM, int WGN, int NS, bool STATS = false>
__device__ __forceinline__ void gemm_loop(const u16* __restrict__ A, int lda, const u16* __restrict__ B, int ldb,
                                          int nk, char* lds, f32x4 (&acc)[BM / WGM / 16][BN / WGN / 16],
                                          float* rs = nullptr, float* rq = nullptr, float* cs = nullptr) {
  constexpr int NW = WGM * WGN;
  constexpr int MT = BM / WGM / 16, NT = BN / WGN / 16;
  constexpr int NA = BM / 8 / NW, NB = BN / 8 / NW, PER = NA + NB;
  constexpr int ABYTES = BM * 128, BBYTES = 64 * BN * 2, SBYTES = ABYTES + BBYTES;
  const int tid = threadIdx.x, lane = tid & 63;
  const int wid = __builtin_amdgcn_readfirstlane(tid >> 6);
  const int wm = wid / WGN, wn = wid % WGN;
  const int g = lane >> 4, li = lane & 15;

  const u16* pa[NA];
  const u16* pb[NB];
#pragma unroll
  for (int j = 0; j < NA; ++j) {
    int row = (wid * NA + j) * 8 + (lane >> 3);
    int lc = (lane & 7) ^ ((row >> 1) & 7);
    pa[j] = A + (long)row * lda + lc * 8;
  }
#pragma unroll
  for (int j = 0; j < NB; ++j) {
    constexpr int CPR = BN / 8, RPI = 64 / CPR;
    int kr = (wid * NB + j) * RPI + lane / CPR;
    int lc = (lane % CPR) ^ swzB<BN>(kr);
    pb[j] = B + (long)kr * ldb + lc * 8;
  }
  auto issue = [&](int kt) {
    char* sb = lds + (kt % NS) * SBYTES;
#pragma unroll
    for (int j = 0; j < NA; ++j)
      __builtin_amdgcn_global_load_lds((const unsigned*)(pa[j] + kt * 64),
                                       (unsigned*)(sb + (wid * NA + j) * 1024 + lane * 16), 16, 0, 0);
#pragma unroll
    for (int j = 0; j < NB; ++j)
      __builtin_amdgcn_global_load_lds((const unsigned*)(pb[j] + (long)kt * 64 * ldb),
                                       (unsigned*)(sb + ABYTES + (wid * NB + j) * 1024 + lane * 16), 16, 0, 0);
  };
  static_assert(NS == 3, "ring of 3");
  auto rdfrag = [&](int kt, int kk, bf16x8 (&af)[MT], bf16x8 (&bfr)[NT]) {
    const char* cA = lds + (kt % NS) * SBYTES;
    const char* cB = cA + ABYTES;
#pragma unroll
    for (int mt = 0; mt < MT; ++mt) {
      int row = wm * (MT * 16) + mt * 16 + li, ch = kk * 4 + g;
      af[mt] = *reinterpret_cast<const bf16x8*>(cA + row * 128 + ((ch ^ ((row >> 1) & 7)) << 4));
    }
#pragma unroll
    for (int nt = 0; nt < NT; ++nt) {
      int col = wn * (NT * 16) + nt * 16 + 4 * (li & 3);
      int ch = col >> 3, hb = (li & 1) * 8;
      int k0 = kk * 32 + 8 * g + (li >> 2), k1 = k0 + 4;
      s16x4 lo = __builtin_amdgcn_ds_read_tr16_b64_v4i16(
          (lds_s16x4*)(cB + k0 * (BN * 2) + ((ch ^ swzB<BN>(k0)) << 4) + hb));
      s16x4 hi = __builtin_amdgcn_ds_read_tr16_b64_v4i16(
          (lds_s16x4*)(cB + k1 * (BN * 2) + ((ch ^ swzB<BN>(k1)) << 4) + hb));
      bfr[nt] = bf16x8{lo[0], lo[1], lo[2], lo[3], hi[0], hi[1], hi[2], hi[3]};
    }
  };
  auto mma = [&](bf16x8 (&af)[MT], bf16x8 (&bfr)[NT], int m0, int m1) {
#pragma unroll
    for (int mt = m0; mt < m1; ++mt)
#pragma unroll
      for (int nt = 0; nt < NT; ++nt)
        acc[mt][nt] = __builtin_amdgcn_mfma_f32_16x16x32_bf16(bfr[nt], af[mt], acc[mt][nt], 0, 0, 0);
  };
  auto stats = [&](bf16x8 (&af)[MT], bf16x8 (&bfr)[NT]) {
    if constexpr (STATS) {
      if (wn == 0) {
#pragma unroll
        for (int mt = 0; mt < MT; ++mt) acc_stats(af[mt], rs[mt], rq[mt]);
      }
      if (wm == 0) {
#pragma unroll
        for (int nt = 0; nt < NT; ++nt) acc_sum(bfr[nt], cs[nt]);
      }
    }
  };
  bf16x8 a0[MT], b0[NT], a1[MT], b1[NT];
  issue(0); issue(1); issue(2);
  wait_vm<2 * PER>();
  __builtin_amdgcn_s_barrier();
  asm volatile("" ::: "memory");
  rdfrag(0, 0, a0, b0);
  for (int kt = 0; kt < nk; ++kt) {
    mma(a0, b0, 0, 1);
    __builtin_amdgcn_sched_barrier(0);
    rdfrag(kt, 1, a1, b1);
    __builtin_amdgcn_sched_barrier(0);
    mma(a0, b0, 1, MT);
    stats(a0, b0);
    __builtin_amdgcn_sched_barrier(0);
    if (kt + 1 < nk) {
      __builtin_amdgcn_s_waitcnt(0xC07F);
      if (kt + 2 < nk) wait_vm<PER>(); else wait_vm<0>();
      __builtin_amdgcn_s_barrier();
      asm volatile("" ::: "memory");
      if (kt + 3 < nk) issue(kt + 3);
      rdfrag(kt + 1, 0, a0, b0);
    }
    __builtin_amdgcn_sched_barrier(0);
    mma(a1, b1, 0, MT);
    stats(a1, b1);
    __builtin_amdgcn_sched_barrier(0);
  }
  __builtin_amdgcn_s_barrier();
  asm volatile("" ::: "memory");
}

constexpr int NS1 = 3;
__global__ __launch_bounds__(512) void k_gemm1(const u16* __restrict__ xn, const u16* __restrict__ wqkv,
                                               const u16* __restrict__ w1, u16* __restrict__ QKV,
                                               u16* __restrict__ Hb) {
  __shared__ __attribute__((aligned(16))) char lds[NS1 * 49152];
  const int tile = (blockIdx.x & 7) * 27 + (blockIdx.x >> 3);
  const int bn = tile >> 3, bm = tile & 7;
  const int tid = threadIdx.x, lane = tid & 63;
  const int wid = __builtin_amdgcn_readfirstlane(tid >> 6);
  const int g = lane >> 4, li = lane & 15;
  const int wm = wid >> 1, wn = wid & 1;
  const u16* A = xn + (long)bm * 256 * D_;
  const bool isH = bn >= 15;
  const u16* Bp = isH ? (w1 + (bn - 15) * 128) : (wqkv + bn * 128);
  const int ldb = isH ? FF_ : NQKV_;
  f32x4 acc[4][4];
#pragma unroll
  for (int i = 0; i < 4; ++i)
#pragma unroll
    for (int j = 0; j < 4; ++j) acc[i][j] = f32x4{0.f, 0.f, 0.f, 0.f};
  float rs[4] = {0.f, 0.f, 0.f, 0.f}, rq[4] = {0.f, 0.f, 0.f, 0.f}, cs[4] = {0.f, 0.f, 0.f, 0.f};
  gemm_loop<256, 128, 4, 2, NS1, true>(A, D_, Bp, ldb, D_ / 64, lds, acc, rs, rq, cs);

  float* st = reinterpret_cast<float*>(lds + 8 * 64 * 144);
  if (wn == 0) {
#pragma unroll
    for (int mt = 0; mt < 4; ++mt) {
      float a = rs[mt], b = rq[mt];
      a += __shfl_xor(a, 16); a += __shfl_xor(a, 32);
      b += __shfl_xor(b, 16); b += __shfl_xor(b, 32);
      if (g == 0) {
        float mean = a * (1.0f / D_);
        float var = fmaxf(b * (1.0f / D_) - mean * mean, 0.f);
        float den = rbf(sqrtf(rbf(rbf(var) + 1e-5f)));
        int row = wm * 64 + mt * 16 + li;
        st[row * 2] = rbf(mean);
        st[row * 2 + 1] = 1.0f / den;
      }
    }
  }
  if (wm == 0) {
#pragma unroll
    for (int nt = 0; nt < 4; ++nt) {
      float a = cs[nt];
      a += __shfl_xor(a, 16); a += __shfl_xor(a, 32);
      if (g == 0) st[512 + wn * 64 + nt * 16 + li] = a;
    }
  }
  __syncthreads();
  char* wl = lds + wid * (64 * 144);
  const float lo = isH ? 0.f : -3.0e38f;
#pragma unroll
  for (int mt = 0; mt < 4; ++mt) {
    const int row = wm * 64 + mt * 16 + li;
    const float mean = st[row * 2], rden = st[row * 2 + 1];
#pragma unroll
    for (int nt = 0; nt < 4; ++nt) {
      f32x4 cw = *reinterpret_cast<const f32x4*>(st + 512 + wn * 64 + nt * 16 + 4 * g);
      f32x4 a = acc[mt][nt];
      float v0 = (a[0] - mean * cw[0]) * rden, v1 = (a[1] - mean * cw[1]) * rden;
      float v2 = (a[2] - mean * cw[2]) * rden, v3 = (a[3] - mean * cw[3]) * rden;
      u32x2 o = {pack2(fmaxf(v0, lo), fmaxf(v1, lo)), pack2(fmaxf(v2, lo), fmaxf(v3, lo))};
      *reinterpret_cast<u32x2*>(wl + (mt * 16 + li) * 144 + (nt * 16 + 4 * g) * 2) = o;
    }
  }
  u16* C = isH ? (Hb + (bn - 15) * 128) : (QKV + bn * 128);
  C += (long)(bm * 256 + wm * 64) * ldb + wn * 64;
#pragma unroll
  for (int it = 0; it < 8; ++it) {
    int r = it * 8 + (lane >> 3), c = lane & 7;
    u32x4 v = *reinterpret_cast<const u32x4*>(wl + r * 144 + c * 16);
    *reinterpret_cast<u32x4*>(C + (long)r * ldb + c * 8) = v;
  }
}

__device__ __forceinline__ int kvsw(int row) { return (0x78 >> (2 * ((row >> 2) & 3))) & 3; }

__device__ __forceinline__ void attn_block(const u16* __restrict__ QKV, u16* __restrict__ O, char* lds, int h, int qblk) {
  char* Ks = lds;
  char* Vs = lds + 384 * 64;
  const int tid = threadIdx.x, lane = tid & 63;
  const int wid = __builtin_amdgcn_readfirstlane(tid >> 6);
  const int g = lane >> 4, li = lane & 15;
  const int kbase = qblk * 128 - 256;

  {
    u32x2 kv[12], vv[12];
#pragma unroll
    for (int i = 0; i < 12; ++i) {
      int idx = tid + 256 * i, kr = idx >> 3, sl = idx & 7;
      int key = kbase + kr;
      const bool ok = (sl < 5) && (key >= 0);
      const u16* src = QKV + (long)(key < 0 ? 0 : key) * NQKV_ + 640 + h * DH_ + (sl < 5 ? sl : 0) * 4;
      u32x2 a = *reinterpret_cast<const u32x2*>(src);
      u32x2 b = *reinterpret_cast<const u32x2*>(src + 640);
      kv[i] = u32x2{ok ? a[0] : 0u, ok ? a[1] : 0u};
      vv[i] = u32x2{ok ? b[0] : 0u, ok ? b[1] : 0u};
    }
#pragma unroll
    for (int i = 0; i < 12; ++i) {
      int idx = tid + 256 * i, kr = idx >> 3, sl = idx & 7;
      int off = kr * 64 + (((sl >> 1) ^ kvsw(kr)) << 4) + (sl & 1) * 8;
      *reinterpret_cast<u32x2*>(Ks + off) = kv[i];
      *reinterpret_cast<u32x2*>(Vs + off) = vv[i];
    }
  }
  const int q0 = qblk * 128 + wid * 32;
  bf16x8 qf[2];
#pragma unroll
  for (int a = 0; a < 2; ++a) {
    const u16* src = QKV + (long)(q0 + a * 16 + li) * NQKV_ + h * DH_;
    u32x2 x0 = *reinterpret_cast<const u32x2*>(src + (g < 3 ? g * 8 : 0));
    u32x2 x1 = *reinterpret_cast<const u32x2*>(src + (g < 2 ? g * 8 + 4 : 0));
    u32x4 t = {g < 3 ? x0[0] : 0u, g < 3 ? x0[1] : 0u, g < 2 ? x1[0] : 0u, g < 2 ? x1[1] : 0u};
    qf[a] = __builtin_bit_cast(bf16x8, t);
  }
  __syncthreads();

  const float C = 0.22360679774997896f * 1.4426950408889634f;
  const float NEG = -3e38f;
  const int lb = wid * 32;
  const int base = q0 - 256;
#pragma unroll 1
  for (int a = 0; a < 2; ++a) {
    const bf16x8 qa = a ? qf[1] : qf[0];
    const int q = q0 + a * 16 + li;
    f32x4 s[18];
#pragma unroll
    for (int t = 0; t < 18; ++t) {
      int r = lb + 16 * t + li;
      bf16x8 kf = *reinterpret_cast<const bf16x8*>(Ks + r * 64 + ((g ^ kvsw(r)) << 4));
      f32x4 z = {0.f, 0.f, 0.f, 0.f};
      s[t] = __builtin_amdgcn_mfma_f32_16x16x32_bf16(kf, qa, z, 0, 0, 0);
    }
#pragma unroll
    for (int t = 0; t < 18; ++t) {
      int key0 = base + 16 * t;
      if (t < 2 || t >= 16) {
#pragma unroll
        for (int e = 0; e < 4; ++e) {
          int key = key0 + 4 * g + e;
          bool ok = (key >= 0) && (key <= q) && (q - key < 256);
          s[t][e] = ok ? s[t][e] : NEG;
        }
      } else if (key0 < 0) {
        s[t] = f32x4{NEG, NEG, NEG, NEG};
      }
    }
    float mx = NEG;
#pragma unroll
    for (int t = 0; t < 18; ++t)
#pragma unroll
      for (int e = 0; e < 4; ++e) mx = fmaxf(mx, s[t][e]);
    mx = fmaxf(mx, __shfl_xor(mx, 16));
    mx = fmaxf(mx, __shfl_xor(mx, 32));
    const float mc = -mx * C;
    float sum = 0.f;
    f32x4 o0 = {0.f, 0.f, 0.f, 0.f}, o1 = {0.f, 0.f, 0.f, 0.f};
#pragma unroll
    for (int u = 0; u < 9; ++u) {
      bf16x8 vf[2];
#pragma unroll
      for (int dt = 0; dt < 2; ++dt) {
        int r0 = lb + 32 * u + 4 * g + (li >> 2), r1 = r0 + 16;
        int ch = dt * 2 + ((li & 3) >> 1), hb = (li & 1) * 8;
        s16x4 lo = __builtin_amdgcn_ds_read_tr16_b64_v4i16((lds_s16x4*)(Vs + r0 * 64 + ((ch ^ kvsw(r0)) << 4) + hb));
        s16x4 hi = __builtin_amdgcn_ds_read_tr16_b64_v4i16((lds_s16x4*)(Vs + r1 * 64 + ((ch ^ kvsw(r1)) << 4) + hb));
        vf[dt] = bf16x8{lo[0], lo[1], lo[2], lo[3], hi[0], hi[1], hi[2], hi[3]};
      }
      float p[8];
#pragma unroll
      for (int e = 0; e < 4; ++e) {
        p[e] = __builtin_amdgcn_exp2f(fmaf(s[2 * u][e], C, mc));
        p[4 + e] = __builtin_amdgcn_exp2f(fmaf(s[2 * u + 1][e], C, mc));
      }
#pragma unroll
      for (int e = 0; e < 8; ++e) sum += p[e];
      u32x4 pb = {pack2(p[0], p[1]), pack2(p[2], p[3]), pack2(p[4], p[5]), pack2(p[6], p[7])};
      bf16x8 pf = __builtin_bit_cast(bf16x8, pb);
      o0 = __builtin_amdgcn_mfma_f32_16x16x32_bf16(vf[0], pf, o0, 0, 0, 0);
      o1 = __builtin_amdgcn_mfma_f32_16x16x32_bf16(vf[1], pf, o1, 0, 0, 0);
    }
    sum += __shfl_xor(sum, 16);
    sum += __shfl_xor(sum, 32);
    float inv = 1.0f / sum;
    u16* dst = O + (long)q * D_ + h * DH_;
    u32x2 w0 = {pack2(o0[0] * inv, o0[1] * inv), pack2(o0[2] * inv, o0[3] * inv)};
    *reinterpret_cast<u32x2*>(dst + 4 * g) = w0;
    if (g == 0) {
      u32x2 w1 = {pack2(o1[0] * inv, o1[1] * inv), pack2(o1[2] * inv, o1[3] * inv)};
      *reinterpret_cast<u32x2*>(dst + 16) = w1;
    }
  }
}

__global__ __launch_bounds__(256, 2) void k_mid(const u16* __restrict__ QKV, u16* __restrict__ O,
                                                const u16* __restrict__ Hb, const u16* __restrict__ w2,
                                                u16* __restrict__ Fb) {
  __shared__ __attribute__((aligned(16))) char lds[3 * 24576];
  const int xcd = blockIdx.x & 7, loc = blockIdx.x >> 3;
  if (loc >= 20) {
    const int idx = loc - 20;
    attn_block(QKV, O, lds, xcd * 4 + (idx >> 4), idx & 15);
    return;
  }
  const int bm = xcd * 2 + (loc & 1);
  const int bn = loc >> 1;
  const int tid = threadIdx.x, lane = tid & 63;
  const int wid = __builtin_amdgcn_readfirstlane(tid >> 6);
  const int g = lane >> 4, li = lane & 15, wm = wid >> 1, wn = wid & 1;
  f32x4 acc[4][2];
#pragma unroll
  for (int i = 0; i < 4; ++i) { acc[i][0] = f32x4{0.f, 0.f, 0.f, 0.f}; acc[i][1] = f32x4{0.f, 0.f, 0.f, 0.f}; }
  gemm_loop<128, 64, 2, 2, 3>(Hb + (long)bm * 128 * FF_, FF_, w2 + bn * 64, D_, FF_ / 64, lds, acc);
#pragma unroll
  for (int mt = 0; mt < 4; ++mt)
#pragma unroll
    for (int nt = 0; nt < 2; ++nt) {
      int m = bm * 128 + wm * 64 + mt * 16 + li;
      int n = bn * 64 + wn * 32 + nt * 16 + 4 * g;
      u32x2 o = {pack2(acc[mt][nt][0], acc[mt][nt][1]), pack2(acc[mt][nt][2], acc[mt][nt][3])};
      *reinterpret_cast<u32x2*>(Fb + (long)m * D_ + n) = o;
    }
}

__global__ __launch_bounds__(256) void k_gemm2(const u16* __restrict__ x, const u16* __restrict__ O,
                                               const u16* __restrict__ wo, const u16* __restrict__ Fb,
                                               u16* __restrict__ out) {
  __shared__ __attribute__((aligned(16))) char lds[3 * 16384];
  const int loc = blockIdx.x >> 3;
  const int bm = (blockIdx.x & 7) * 4 + (loc & 3), bn = loc >> 2;
  const int tid = threadIdx.x, lane = tid & 63, wid = tid >> 6;
  const int g = lane >> 4, li = lane & 15, wm = wid >> 1, wn = wid & 1;
  u32x2 xv[2][2], fq[2][2];
#pragma unroll
  for (int mt = 0; mt < 2; ++mt)
#pragma unroll
    for (int nt = 0; nt < 2; ++nt) {
      int n = bn * 64 + wn * 32 + nt * 16 + 4 * g;
      int m = bm * 64 + wm * 32 + mt * 16 + li;
      long off = (long)m * D_ + n;
      xv[mt][nt] = *reinterpret_cast<const u32x2*>(x + off);
      fq[mt][nt] = *reinterpret_cast<const u32x2*>(Fb + off);
    }
  f32x4 as[2][2];
#pragma unroll
  for (int i = 0; i < 2; ++i)
#pragma unroll
    for (int j = 0; j < 2; ++j) as[i][j] = f32x4{0.f, 0.f, 0.f, 0.f};
  gemm_loop<64, 64, 2, 2, 3>(O + (long)bm * 64 * D_, D_, wo + bn * 64, D_, D_ / 64, lds, as);
#pragma unroll
  for (int mt = 0; mt < 2; ++mt)
#pragma unroll
    for (int nt = 0; nt < 2; ++nt) {
      int n = bn * 64 + wn * 32 + nt * 16 + 4 * g;
      int m = bm * 64 + wm * 32 + mt * 16 + li;
      u32x2 xq = xv[mt][nt];
      float xs[4] = {__uint_as_float(xq[0] << 16), __uint_as_float(xq[0] & 0xffff0000u),
                     __uint_as_float(xq[1] << 16), __uint_as_float(xq[1] & 0xffff0000u)};
      u32x2 fz = fq[mt][nt];
      float fs[4] = {__uint_as_float(fz[0] << 16), __uint_as_float(fz[0] & 0xffff0000u),
                     __uint_as_float(fz[1] << 16), __uint_as_float(fz[1] & 0xffff0000u)};
      float r[4];
#pragma unroll
      for (int e = 0; e < 4; ++e) {
        float sv = rbf(as[mt][nt][e]);
        float f = fs[e];
        float gg = rbf(f / (1.0f + __expf(-f)));
        r[e] = xs[e] + rbf(sv * gg);
      }
      u32x2 o = {pack2(r[0], r[1]), pack2(r[2], r[3])};
      *reinterpret_cast<u32x2*>(out + (long)m * D_ + n) = o;
    }
}

extern "C" void kernel_launch(void* const* d_in, const int* in_sizes, int n_in, void* d_out, int out_size,
                              void* d_ws, size_t ws_size, hipStream_t stream) {
  const u16* x = (const u16*)d_in[0];
  const u16* wqkv = (const u16*)d_in[1];
  const u16* wo = (const u16*)d_in[2];
  const u16* w1 = (const u16*)d_in[4];
  const u16* w2 = (const u16*)d_in[5];
  u16* out = (u16*)d_out;
  char* ws = (char*)d_ws;
  u16* QKV = (u16*)(ws);
  u16* Hb = (u16*)(ws + (8l << 20));
  u16* O = (u16*)(ws + (16l << 20));
  k_gemm1<<<8 * 27, 512, 0, stream>>>(x, wqkv, w1, QKV, Hb);
  u16* Fb = (u16*)(ws + (24l << 20));
  k_mid<<<8 * 84, 256, 0, stream>>>(QKV, O, Hb, w2, Fb);
  k_gemm2<<<32 * 10, 256, 0, stream>>>(x, O, wo, Fb, out);
}
```

```cpp
#include <hip/hip_runtime.h>
#include <hip/hip_bf16.h>


typedef unsigned short u16;
using bf16x8 = __attribute__((ext_vector_type(8))) short;
using s16x4  = __attribute__((ext_vector_type(4))) short;
using f32x4  = __attribute__((ext_vector_type(4))) float;
using u32x4  = __attribute__((ext_vector_type(4))) unsigned;
using u32x2  = __attribute__((ext_vector_type(2))) unsigned;
typedef __attribute__((address_space(3))) s16x4 lds_s16x4;

constexpr int S_ = 2048, D_ = 640, H_ = 32, DH_ = 20, FF_ = 1536, NQKV_ = 1920;

__device__ __forceinline__ float bf2f(u16 v) { return __uint_as_float(((unsigned)v) << 16); }
__device__ __forceinline__ u16 f2bf(float f) {
  __bf16 b = (__bf16)f;
  return __builtin_bit_cast(u16, b);
}
__device__ __forceinline__ float rbf(float f) { return bf2f(f2bf(f)); }
typedef __attribute__((ext_vector_type(2))) float f32x2;
typedef __attribute__((ext_vector_type(2))) __bf16 bf16x2_t;
__device__ __forceinline__ unsigned pack2(float a, float b) {
  f32x2 v = {a, b};
  return __builtin_bit_cast(unsigned, __builtin_convertvector(v, bf16x2_t));
}

template <int BN> __device__ __forceinline__ int swzB(int k) {
  if constexpr (BN == 128) return ((k & 3) | ((k >> 1) & 4)) << 1;
  else return (((k >> 1) & 1) | ((k >> 2) & 2)) << 1;
}

template <int N> __device__ __forceinline__ void wait_vm() { asm volatile("s_waitcnt vmcnt(%0)" ::"i"(N) : "memory"); }

template <int BM, int BN, int WGM, int WGN, int NS>
__device__ __forceinline__ void gemm_loop(const u16* __restrict__ A, int lda, const u16* __restrict__ B, int ldb,
                                          int nk, char* lds, f32x4 (&acc)[BM / WGM / 16][BN / WGN / 16]) {
  constexpr int NW = WGM * WGN;
  constexpr int MT = BM / WGM / 16, NT = BN / WGN / 16;
  constexpr int NA = BM / 8 / NW, NB = BN / 8 / NW, PER = NA + NB;
  constexpr int ABYTES = BM * 128, BBYTES = 64 * BN * 2, SBYTES = ABYTES + BBYTES;
  const int tid = threadIdx.x, lane = tid & 63;
  const int wid = __builtin_amdgcn_readfirstlane(tid >> 6);
  const int wm = wid / WGN, wn = wid % WGN;
  const int g = lane >> 4, li = lane & 15;

  const u16* pa[NA];
  const u16* pb[NB];
#pragma unroll
  for (int j = 0; j < NA; ++j) {
    int row = (wid * NA + j) * 8 + (lane >> 3);
    int lc = (lane & 7) ^ ((row >> 1) & 7);
    pa[j] = A + (long)row * lda + lc * 8;
  }
#pragma unroll
  for (int j = 0; j < NB; ++j) {
    constexpr int CPR = BN / 8, RPI = 64 / CPR;
    int kr = (wid * NB + j) * RPI + lane / CPR;
    int lc = (lane % CPR) ^ swzB<BN>(kr);
    pb[j] = B + (long)kr * ldb + lc * 8;
  }
  auto issue = [&](int kt) {
    char* sb = lds + (kt % NS) * SBYTES;
#pragma unroll
    for (int j = 0; j < NA; ++j)
      __builtin_amdgcn_global_load_lds((const unsigned*)(pa[j] + kt * 64),
                                       (unsigned*)(sb + (wid * NA + j) * 1024 + lane * 16), 16, 0, 0);
#pragma unroll
    for (int j = 0; j < NB; ++j)
      __builtin_amdgcn_global_load_lds((const unsigned*)(pb[j] + (long)kt * 64 * ldb),
                                       (unsigned*)(sb + ABYTES + (wid * NB + j) * 1024 + lane * 16), 16, 0, 0);
  };
  static_assert(NS == 3, "ring of 3");
  auto rdfrag = [&](int kt, int kk, bf16x8 (&af)[MT], bf16x8 (&bfr)[NT]) {
    const char* cA = lds + (kt % NS) * SBYTES;
    const char* cB = cA + ABYTES;
#pragma unroll
    for (int mt = 0; mt < MT; ++mt) {
      int row = wm * (MT * 16) + mt * 16 + li, ch = kk * 4 + g;
      af[mt] = *reinterpret_cast<const bf16x8*>(cA + row * 128 + ((ch ^ ((row >> 1) & 7)) << 4));
    }
#pragma unroll
    for (int nt = 0; nt < NT; ++nt) {
      int col = wn * (NT * 16) + nt * 16 + 4 * (li & 3);
      int ch = col >> 3, hb = (li & 1) * 8;
      int k0 = kk * 32 + 8 * g + (li >> 2), k1 = k0 + 4;
      s16x4 lo = __builtin_amdgcn_ds_read_tr16_b64_v4i16(
          (lds_s16x4*)(cB + k0 * (BN * 2) + ((ch ^ swzB<BN>(k0)) << 4) + hb));
      s16x4 hi = __builtin_amdgcn_ds_read_tr16_b64_v4i16(
          (lds_s16x4*)(cB + k1 * (BN * 2) + ((ch ^ swzB<BN>(k1)) << 4) + hb));
      bfr[nt] = bf16x8{lo[0], lo[1], lo[2], lo[3], hi[0], hi[1], hi[2], hi[3]};
    }
  };
  auto mma = [&](bf16x8 (&af)[MT], bf16x8 (&bfr)[NT], int m0, int m1) {
#pragma unroll
    for (int mt = m0; mt < m1; ++mt)
#pragma unroll
      for (int nt = 0; nt < NT; ++nt)
        acc[mt][nt] = __builtin_amdgcn_mfma_f32_16x16x32_bf16(bfr[nt], af[mt], acc[mt][nt], 0, 0, 0);
  };
  bf16x8 a0[MT], b0[NT], a1[MT], b1[NT];
  issue(0); issue(1); issue(2);
  wait_vm<2 * PER>();
  __builtin_amdgcn_s_barrier();
  asm volatile("" ::: "memory");
  rdfrag(0, 0, a0, b0);
  for (int kt = 0; kt < nk; ++kt) {
    mma(a0, b0, 0, 1);
    __builtin_amdgcn_sched_barrier(0);
    rdfrag(kt, 1, a1, b1);
    __builtin_amdgcn_sched_barrier(0);
    mma(a0, b0, 1, MT);
    __builtin_amdgcn_sched_barrier(0);
    if (kt + 1 < nk) {
      __builtin_amdgcn_s_waitcnt(0xC07F);
      if (kt + 2 < nk) wait_vm<PER>(); else wait_vm<0>();
      __builtin_amdgcn_s_barrier();
      asm volatile("" ::: "memory");
      if (kt + 3 < nk) issue(kt + 3);
      rdfrag(kt + 1, 0, a0, b0);
    }
    __builtin_amdgcn_sched_barrier(0);
    mma(a1, b1, 0, MT);
    __builtin_amdgcn_sched_barrier(0);
  }
  __builtin_amdgcn_s_barrier();
  asm volatile("" ::: "memory");
}

__global__ __launch_bounds__(64) void k_ln(const u16* __restrict__ x, u16* __restrict__ xn) {

  const int lane = threadIdx.x & 63, li = lane & 15;
  const int row = blockIdx.x * 4 + (threadIdx.x >> 4);
  float v[40];
#pragma unroll
  for (int c = 0; c < 5; ++c) {
    u32x4 q = *reinterpret_cast<const u32x4*>(x + (long)row * D_ + (c * 16 + li) * 8);
#pragma unroll
    for (int e = 0; e < 4; ++e) {
      v[c * 8 + 2 * e] = __uint_as_float(q[e] << 16);
      v[c * 8 + 2 * e + 1] = __uint_as_float(q[e] & 0xffff0000u);
    }
  }
  float s = 0.f;
#pragma unroll
  for (int e = 0; e < 40; ++e) s += v[e];
#pragma unroll
  for (int o = 1; o < 16; o <<= 1) s += __shfl_xor(s, o);
  float mean = s * (1.0f / D_);
  float q2 = 0.f;
#pragma unroll
  for (int e = 0; e < 40; ++e) { float d = v[e] - mean; q2 += d * d; }
#pragma unroll
  for (int o = 1; o < 16; o <<= 1) q2 += __shfl_xor(q2, o);
  float var = rbf(q2 * (1.0f / D_));
  float mb = rbf(mean);
  float den = rbf(sqrtf(rbf(var + 1e-5f)));
  float rden = 1.0f / den;
#pragma unroll
  for (int c = 0; c < 5; ++c) {
    u32x4 o;
#pragma unroll
    for (int e = 0; e < 4; ++e)
      o[e] = pack2(rbf(v[c * 8 + 2 * e] - mb) * rden, rbf(v[c * 8 + 2 * e + 1] - mb) * rden);
    *reinterpret_cast<u32x4*>(xn + (long)row * D_ + (c * 16 + li) * 8) = o;
  }
}

constexpr int NS1 = 3;
__global__ __launch_bounds__(512) void k_gemm1(const u16* __restrict__ xn, const u16* __restrict__ wqkv,
                                               const u16* __restrict__ w1, u16* __restrict__ QKV,
                                               u16* __restrict__ Hb) {
  __shared__ __attribute__((aligned(16))) char lds[NS1 * 49152];
  const int tile = (blockIdx.x & 7) * 27 + (blockIdx.x >> 3);
  const int bn = tile >> 3, bm = tile & 7;
  const int tid = threadIdx.x, lane = tid & 63;
  const int wid = __builtin_amdgcn_readfirstlane(tid >> 6);
  const int g = lane >> 4, li = lane & 15;
  const int wm = wid >> 1, wn = wid & 1;
  const u16* A = xn + (long)bm * 256 * D_;
  const bool isH = bn >= 15;
  const u16* Bp = isH ? (w1 + (bn - 15) * 128) : (wqkv + bn * 128);
  const int ldb = isH ? FF_ : NQKV_;
  f32x4 acc[4][4];
#pragma unroll
  for (int i = 0; i < 4; ++i)
#pragma unroll
    for (int j = 0; j < 4; ++j) acc[i][j] = f32x4{0.f, 0.f, 0.f, 0.f};
  gemm_loop<256, 128, 4, 2, NS1>(A, D_, Bp, ldb, D_ / 64, lds, acc);

  char* wl = lds + wid * (64 * 144);
  const float lo = isH ? 0.f : -3.0e38f;
#pragma unroll
  for (int mt = 0; mt < 4; ++mt)
#pragma unroll
    for (int nt = 0; nt < 4; ++nt) {
      f32x4 a = acc[mt][nt];
      u32x2 o = {pack2(fmaxf(a[0], lo), fmaxf(a[1], lo)), pack2(fmaxf(a[2], lo), fmaxf(a[3], lo))};
      *reinterpret_cast<u32x2*>(wl + (mt * 16 + li) * 144 + (nt * 16 + 4 * g) * 2) = o;
    }
  u16* C = isH ? (Hb + (bn - 15) * 128) : (QKV + bn * 128);
  C += (long)(bm * 256 + wm * 64) * ldb + wn * 64;
#pragma unroll
  for (int it = 0; it < 8; ++it) {
    int r = it * 8 + (lane >> 3), c = lane & 7;
    u32x4 v = *reinterpret_cast<const u32x4*>(wl + r * 144 + c * 16);
    *reinterpret_cast<u32x4*>(C + (long)r * ldb + c * 8) = v;
  }
}

__device__ __forceinline__ int kvsw(int row) { return (0x78 >> (2 * ((row >> 2) & 3))) & 3; }

__device__ __forceinline__ void attn_block(const u16* __restrict__ QKV, u16* __restrict__ O, char* lds, int h, int qblk) {
  char* Ks = lds;
  char* Vs = lds + 384 * 64;
  const int tid = threadIdx.x, lane = tid & 63;
  const int wid = __builtin_amdgcn_readfirstlane(tid >> 6);
  const int g = lane >> 4, li = lane & 15;
  const int kbase = qblk * 128 - 256;

  {
    u32x2 kv[12], vv[12];
#pragma unroll
    for (int i = 0; i < 12; ++i) {
      int idx = tid + 256 * i, kr = idx >> 3, sl = idx & 7;
      int key = kbase + kr;
      const bool ok = (sl < 5) && (key >= 0);
      const u16* src = QKV + (long)(key < 0 ? 0 : key) * NQKV_ + 640 + h * DH_ + (sl < 5 ? sl : 0) * 4;
      u32x2 a = *reinterpret_cast<const u32x2*>(src);
      u32x2 b = *reinterpret_cast<const u32x2*>(src + 640);
      kv[i] = u32x2{ok ? a[0] : 0u, ok ? a[1] : 0u};
      vv[i] = u32x2{ok ? b[0] : 0u, ok ? b[1] : 0u};
    }
#pragma unroll
    for (int i = 0; i < 12; ++i) {
      int idx = tid + 256 * i, kr = idx >> 3, sl = idx & 7;
      int off = kr * 64 + (((sl >> 1) ^ kvsw(kr)) << 4) + (sl & 1) * 8;
      *reinterpret_cast<u32x2*>(Ks + off) = kv[i];
      *reinterpret_cast<u32x2*>(Vs + off) = vv[i];
    }
  }
  const int q0 = qblk * 128 + wid * 32;
  bf16x8 qf[2];
#pragma unroll
  for (int a = 0; a < 2; ++a) {
    const u16* src = QKV + (long)(q0 + a * 16 + li) * NQKV_ + h * DH_;
    u32x2 x0 = *reinterpret_cast<const u32x2*>(src + (g < 3 ? g * 8 : 0));
    u32x2 x1 = *reinterpret_cast<const u32x2*>(src + (g < 2 ? g * 8 + 4 : 0));
    u32x4 t = {g < 3 ? x0[0] : 0u, g < 3 ? x0[1] : 0u, g < 2 ? x1[0] : 0u, g < 2 ? x1[1] : 0u};
    qf[a] = __builtin_bit_cast(bf16x8, t);
  }
  __syncthreads();

  const float C = 0.22360679774997896f * 1.4426950408889634f;
  const float NEG = -3e38f;
  const int lb = wid * 32;
  const int base = q0 - 256;
#pragma unroll 1
  for (int a = 0; a < 2; ++a) {
    const bf16x8 qa = a ? qf[1] : qf[0];
    const int q = q0 + a * 16 + li;
    f32x4 s[18];
#pragma unroll
    for (int t = 0; t < 18; ++t) {
      int r = lb + 16 * t + li;
      bf16x8 kf = *reinterpret_cast<const bf16x8*>(Ks + r * 64 + ((g ^ kvsw(r)) << 4));
      f32x4 z = {0.f, 0.f, 0.f, 0.f};
      s[t] = __builtin_amdgcn_mfma_f32_16x16x32_bf16(kf, qa, z, 0, 0, 0);
      if (t % 6 == 5) __builtin_amdgcn_sched_barrier(0);
    }
#pragma unroll
    for (int t = 0; t < 18; ++t) {
      int key0 = base + 16 * t;
      if (t < 2 || t >= 16) {
#pragma unroll
        for (int e = 0; e < 4; ++e) {
          int key = key0 + 4 * g + e;
          bool ok = (key >= 0) && (key <= q) && (q - key < 256);
          s[t][e] = ok ? s[t][e] : NEG;
        }
      } else if (key0 < 0) {
        s[t] = f32x4{NEG, NEG, NEG, NEG};
      }
    }
    float mx = NEG;
#pragma unroll
    for (int t = 0; t < 18; ++t)
#pragma unroll
      for (int e = 0; e < 4; ++e) mx = fmaxf(mx, s[t][e]);
    mx = fmaxf(mx, __shfl_xor(mx, 16));
    mx = fmaxf(mx, __shfl_xor(mx, 32));
    const float mc = -mx * C;
    float sum = 0.f;
    f32x4 o0 = {0.f, 0.f, 0.f, 0.f}, o1 = {0.f, 0.f, 0.f, 0.f};
#pragma unroll
    for (int u = 0; u < 9; ++u) {
      bf16x8 vf[2];
#pragma unroll
      for (int dt = 0; dt < 2; ++dt) {
        int r0 = lb + 32 * u + 4 * g + (li >> 2), r1 = r0 + 16;
        int ch = dt * 2 + ((li & 3) >> 1), hb = (li & 1) * 8;
        s16x4 lo = __builtin_amdgcn_ds_read_tr16_b64_v4i16((lds_s16x4*)(Vs + r0 * 64 + ((ch ^ kvsw(r0)) << 4) + hb));
        s16x4 hi = __builtin_amdgcn_ds_read_tr16_b64_v4i16((lds_s16x4*)(Vs + r1 * 64 + ((ch ^ kvsw(r1)) << 4) + hb));
        vf[dt] = bf16x8{lo[0], lo[1], lo[2], lo[3], hi[0], hi[1], hi[2], hi[3]};
      }
      float p[8];
#pragma unroll
      for (int e = 0; e < 4; ++e) {
        p[e] = __builtin_amdgcn_exp2f(fmaf(s[2 * u][e], C, mc));
        p[4 + e] = __builtin_amdgcn_exp2f(fmaf(s[2 * u + 1][e], C, mc));
      }
#pragma unroll
      for (int e = 0; e < 8; ++e) sum += p[e];
      u32x4 pb = {pack2(p[0], p[1]), pack2(p[2], p[3]), pack2(p[4], p[5]), pack2(p[6], p[7])};
      bf16x8 pf = __builtin_bit_cast(bf16x8, pb);
      o0 = __builtin_amdgcn_mfma_f32_16x16x32_bf16(vf[0], pf, o0, 0, 0, 0);
      o1 = __builtin_amdgcn_mfma_f32_16x16x32_bf16(vf[1], pf, o1, 0, 0, 0);
    }
    sum += __shfl_xor(sum, 16);
    sum += __shfl_xor(sum, 32);
    float inv = 1.0f / sum;
    u16* dst = O + (long)q * D_ + h * DH_;
    u32x2 w0 = {pack2(o0[0] * inv, o0[1] * inv), pack2(o0[2] * inv, o0[3] * inv)};
    *reinterpret_cast<u32x2*>(dst + 4 * g) = w0;
    if (g == 0) {
      u32x2 w1 = {pack2(o1[0] * inv, o1[1] * inv), pack2(o1[2] * inv, o1[3] * inv)};
      *reinterpret_cast<u32x2*>(dst + 16) = w1;
    }
  }
}

__global__ __launch_bounds__(256, 3) void k_mid(const u16* __restrict__ QKV, u16* __restrict__ O,
                                                const u16* __restrict__ Hb, const u16* __restrict__ w2,
                                                u16* __restrict__ Fb) {
  __shared__ __attribute__((aligned(16))) char lds[3 * 16384];
  const int xcd = blockIdx.x & 7, loc = blockIdx.x >> 3;
  if (loc >= 40) {
    const int idx = loc - 40;
    attn_block(QKV, O, lds, xcd * 4 + (idx >> 4), idx & 15);
    return;
  }
  const int bm = xcd * 4 + (loc & 3);
  const int bn = loc >> 2;
  const int tid = threadIdx.x, lane = tid & 63;
  const int wid = __builtin_amdgcn_readfirstlane(tid >> 6);
  const int g = lane >> 4, li = lane & 15, wm = wid >> 1, wn = wid & 1;
  f32x4 acc[2][2];
#pragma unroll
  for (int i = 0; i < 2; ++i) { acc[i][0] = f32x4{0.f, 0.f, 0.f, 0.f}; acc[i][1] = f32x4{0.f, 0.f, 0.f, 0.f}; }
  gemm_loop<64, 64, 2, 2, 3>(Hb + (long)bm * 64 * FF_, FF_, w2 + bn * 64, D_, FF_ / 64, lds, acc);
#pragma unroll
  for (int mt = 0; mt < 2; ++mt)
#pragma unroll
    for (int nt = 0; nt < 2; ++nt) {
      int m = bm * 64 + wm * 32 + mt * 16 + li;
      int n = bn * 64 + wn * 32 + nt * 16 + 4 * g;
      u32x2 o = {pack2(acc[mt][nt][0], acc[mt][nt][1]), pack2(acc[mt][nt][2], acc[mt][nt][3])};
      *reinterpret_cast<u32x2*>(Fb + (long)m * D_ + n) = o;
    }
}

__global__ __launch_bounds__(256) void k_gemm2(const u16* __restrict__ x, const u16* __restrict__ O,
                                               const u16* __restrict__ wo, const u16* __restrict__ Fb,
                                               u16* __restrict__ out) {
  __shared__ __attribute__((aligned(16))) char lds[3 * 16384];
  const int loc = blockIdx.x >> 3;
  const int bm = (blockIdx.x & 7) * 4 + (loc & 3), bn = loc >> 2;
  const int tid = threadIdx.x, lane = tid & 63, wid = tid >> 6;
  const int g = lane >> 4, li = lane & 15, wm = wid >> 1, wn = wid & 1;
  u32x2 xv[2][2], fq[2][2];
#pragma unroll
  for (int mt = 0; mt < 2; ++mt)
#pragma unroll
    for (int nt = 0; nt < 2; ++nt) {
      int n = bn * 64 + wn * 32 + nt * 16 + 4 * g;
      int m = bm * 64 + wm * 32 + mt * 16 + li;
      long off = (long)m * D_ + n;
      xv[mt][nt] = *reinterpret_cast<const u32x2*>(x + off);
      fq[mt][nt] = *reinterpret_cast<const u32x2*>(Fb + off);
    }
  f32x4 as[2][2];
#pragma unroll
  for (int i = 0; i < 2; ++i)
#pragma unroll
    for (int j = 0; j < 2; ++j) as[i][j] = f32x4{0.f, 0.f, 0.f, 0.f};
  gemm_loop<64, 64, 2, 2, 3>(O + (long)bm * 64 * D_, D_, wo + bn * 64, D_, D_ / 64, lds, as);
#pragma unroll
  for (int mt = 0; mt < 2; ++mt)
#pragma unroll
    for (int nt = 0; nt < 2; ++nt) {
      int n = bn * 64 + wn * 32 + nt * 16 + 4 * g;
      int m = bm * 64 + wm * 32 + mt * 16 + li;
      u32x2 xq = xv[mt][nt];
      float xs[4] = {__uint_as_float(xq[0] << 16), __uint_as_float(xq[0] & 0xffff0000u),
                     __uint_as_float(xq[1] << 16), __uint_as_float(xq[1] & 0xffff0000u)};
      u32x2 fz = fq[mt][nt];
      float fs[4] = {__uint_as_float(fz[0] << 16), __uint_as_float(fz[0] & 0xffff0000u),
                     __uint_as_float(fz[1] << 16), __uint_as_float(fz[1] & 0xffff0000u)};
      float r[4];
#pragma unroll
      for (int e = 0; e < 4; ++e) {
        float sv = rbf(as[mt][nt][e]);
        float f = fs[e];
        float gg = rbf(f / (1.0f + __expf(-f)));
        r[e] = xs[e] + rbf(sv * gg);
      }
      u32x2 o = {pack2(r[0], r[1]), pack2(r[2], r[3])};
      *reinterpret_cast<u32x2*>(out + (long)m * D_ + n) = o;
    }
}

extern "C" void kernel_launch(void* const* d_in, const int* in_sizes, int n_in, void* d_out, int out_size,
                              void* d_ws, size_t ws_size, hipStream_t stream) {
  const u16* x = (const u16*)d_in[0];
  const u16* wqkv = (const u16*)d_in[1];
  const u16* wo = (const u16*)d_in[2];
  const u16* w1 = (const u16*)d_in[4];
  const u16* w2 = (const u16*)d_in[5];
  u16* out = (u16*)d_out;
  char* ws = (char*)d_ws;
  u16* QKV = (u16*)(ws);
  u16* Hb = (u16*)(ws + (8l << 20));
  u16* O = (u16*)(ws + (16l << 20));
  u16* xn = (u16*)(ws + (20l << 20));
  k_ln<<<512, 64, 0, stream>>>(x, xn);
  k_gemm1<<<8 * 27, 512, 0, stream>>>(xn, wqkv, w1, QKV, Hb);
  u16* Fb = (u16*)(ws + (24l << 20));
  k_mid<<<8 * 104, 256, 0, stream>>>(QKV, O, Hb, w2, Fb);
  k_gemm2<<<32 * 10, 256, 0, stream>>>(x, O, wo, Fb, out);
}
```

```cpp
#include <hip/hip_runtime.h>
#include <hip/hip_bf16.h>


typedef unsigned short u16;
using bf16x8 = __attribute__((ext_vector_type(8))) short;
using s16x4  = __attribute__((ext_vector_type(4))) short;
using f32x4  = __attribute__((ext_vector_type(4))) float;
using u32x4  = __attribute__((ext_vector_type(4))) unsigned;
using u32x2  = __attribute__((ext_vector_type(2))) unsigned;
typedef __attribute__((address_space(3))) s16x4 lds_s16x4;

constexpr int S_ = 2048, D_ = 640, H_ = 32, DH_ = 20, FF_ = 1536, NQKV_ = 1920;

__device__ __forceinline__ float bf2f(u16 v) { return __uint_as_float(((unsigned)v) << 16); }
__device__ __forceinline__ u16 f2bf(float f) {
  __bf16 b = (__bf16)f;
  return __builtin_bit_cast(u16, b);
}
__device__ __forceinline__ float rbf(float f) { return bf2f(f2bf(f)); }
typedef __attribute__((ext_vector_type(2))) float f32x2;
typedef __attribute__((ext_vector_type(2))) __bf16 bf16x2_t;
__device__ __forceinline__ unsigned pack2(float a, float b) {
  f32x2 v = {a, b};
  return __builtin_bit_cast(unsigned, __builtin_convertvector(v, bf16x2_t));
}

template <int BN> __device__ __forceinline__ int swzB(int k) {
  if constexpr (BN == 128) return ((k & 3) | ((k >> 1) & 4)) << 1;
  else return (((k >> 1) & 1) | ((k >> 2) & 2)) << 1;
}

template <int N> __device__ __forceinline__ void wait_vm() { asm volatile("s_waitcnt vmcnt(%0)" ::"i"(N) : "memory"); }

template <int BM, int BN, int WGM, int WGN, int NS>
__device__ __forceinline__ void gemm_loop(const u16* __restrict__ A, int lda, const u16* __restrict__ B, int ldb,
                                          int nk, char* lds, f32x4 (&acc)[BM / WGM / 16][BN / WGN / 16]) {
  constexpr int NW = WGM * WGN;
  constexpr int MT = BM / WGM / 16, NT = BN / WGN / 16;
  constexpr int NA = BM / 8 / NW, NB = BN / 8 / NW, PER = NA + NB;
  constexpr int ABYTES = BM * 128, BBYTES = 64 * BN * 2, SBYTES = ABYTES + BBYTES;
  const int tid = threadIdx.x, lane = tid & 63;
  const int wid = __builtin_amdgcn_readfirstlane(tid >> 6);
  const int wm = wid / WGN, wn = wid % WGN;
  const int g = lane >> 4, li = lane & 15;

  const u16* pa[NA];
  const u16* pb[NB];
#pragma unroll
  for (int j = 0; j < NA; ++j) {
    int row = (wid * NA + j) * 8 + (lane >> 3);
    int lc = (lane & 7) ^ ((row >> 1) & 7);
    pa[j] = A + (long)row * lda + lc * 8;
  }
#pragma unroll
  for (int j = 0; j < NB; ++j) {
    constexpr int CPR = BN / 8, RPI = 64 / CPR;
    int kr = (wid * NB + j) * RPI + lane / CPR;
    int lc = (lane % CPR) ^ swzB<BN>(kr);
    pb[j] = B + (long)kr * ldb + lc * 8;
  }
  auto issue = [&](int kt) {
    char* sb = lds + (kt % NS) * SBYTES;
#pragma unroll
    for (int j = 0; j < NA; ++j)
      __builtin_amdgcn_global_load_lds((const unsigned*)(pa[j] + kt * 64),
                                       (unsigned*)(sb + (wid * NA + j) * 1024 + lane * 16), 16, 0, 0);
#pragma unroll
    for (int j = 0; j < NB; ++j)
      __builtin_amdgcn_global_load_lds((const unsigned*)(pb[j] + (long)kt * 64 * ldb),
                                       (unsigned*)(sb + ABYTES + (wid * NB + j) * 1024 + lane * 16), 16, 0, 0);
  };
  static_assert(NS == 3, "ring of 3");
  auto rdfrag = [&](int kt, int kk, bf16x8 (&af)[MT], bf16x8 (&bfr)[NT]) {
    const char* cA = lds + (kt % NS) * SBYTES;
    const char* cB = cA + ABYTES;
#pragma unroll
    for (int mt = 0; mt < MT; ++mt) {
      int row = wm * (MT * 16) + mt * 16 + li, ch = kk * 4 + g;
      af[mt] = *reinterpret_cast<const bf16x8*>(cA + row * 128 + ((ch ^ ((row >> 1) & 7)) << 4));
    }
#pragma unroll
    for (int nt = 0; nt < NT; ++nt) {
      int col = wn * (NT * 16) + nt * 16 + 4 * (li & 3);
      int ch = col >> 3, hb = (li & 1) * 8;
      int k0 = kk * 32 + 8 * g + (li >> 2), k1 = k0 + 4;
      s16x4 lo = __builtin_amdgcn_ds_read_tr16_b64_v4i16(
          (lds_s16x4*)(cB + k0 * (BN * 2) + ((ch ^ swzB<BN>(k0)) << 4) + hb));
      s16x4 hi = __builtin_amdgcn_ds_read_tr16_b64_v4i16(
          (lds_s16x4*)(cB + k1 * (BN * 2) + ((ch ^ swzB<BN>(k1)) << 4) + hb));
      bfr[nt] = bf16x8{lo[0], lo[1], lo[2], lo[3], hi[0], hi[1], hi[2], hi[3]};
    }
  };
  auto mma = [&](bf16x8 (&af)[MT], bf16x8 (&bfr)[NT], int m0, int m1) {
#pragma unroll
    for (int mt = m0; mt < m1; ++mt)
#pragma unroll
      for (int nt = 0; nt < NT; ++nt)
        acc[mt][nt] = __builtin_amdgcn_mfma_f32_16x16x32_bf16(bfr[nt], af[mt], acc[mt][nt], 0, 0, 0);
  };
  bf16x8 a0[MT], b0[NT], a1[MT], b1[NT];
  issue(0); issue(1); issue(2);
  wait_vm<2 * PER>();
  __builtin_amdgcn_s_barrier();
  asm volatile("" ::: "memory");
  rdfrag(0, 0, a0, b0);
  for (int kt = 0; kt < nk; ++kt) {
    mma(a0, b0, 0, 1);
    __builtin_amdgcn_sched_barrier(0);
    rdfrag(kt, 1, a1, b1);
    __builtin_amdgcn_sched_barrier(0);
    mma(a0, b0, 1, MT);
    __builtin_amdgcn_sched_barrier(0);
    if (kt + 1 < nk) {
      __builtin_amdgcn_s_waitcnt(0xC07F);
      if (kt + 2 < nk) wait_vm<PER>(); else wait_vm<0>();
      __builtin_amdgcn_s_barrier();
      asm volatile("" ::: "memory");
      if (kt + 3 < nk) issue(kt + 3);
      rdfrag(kt + 1, 0, a0, b0);
    }
    __builtin_amdgcn_sched_barrier(0);
    mma(a1, b1, 0, MT);
    __builtin_amdgcn_sched_barrier(0);
  }
  __builtin_amdgcn_s_barrier();
  asm volatile("" ::: "memory");
}

__global__ __launch_bounds__(256) void k_ln(const u16* __restrict__ x, u16* __restrict__ xn) {

  const int lane = threadIdx.x & 63, li = lane & 15;
  const int row = blockIdx.x * 16 + (threadIdx.x >> 4);
  float v[40];
#pragma unroll
  for (int c = 0; c < 5; ++c) {
    u32x4 q = *reinterpret_cast<const u32x4*>(x + (long)row * D_ + (c * 16 + li) * 8);
#pragma unroll
    for (int e = 0; e < 4; ++e) {
      v[c * 8 + 2 * e] = __uint_as_float(q[e] << 16);
      v[c * 8 + 2 * e + 1] = __uint_as_float(q[e] & 0xffff0000u);
    }
  }
  float s = 0.f;
#pragma unroll
  for (int e = 0; e < 40; ++e) s += v[e];
#pragma unroll
  for (int o = 1; o < 16; o <<= 1) s += __shfl_xor(s, o);
  float mean = s * (1.0f / D_);
  float q2 = 0.f;
#pragma unroll
  for (int e = 0; e < 40; ++e) { float d = v[e] - mean; q2 += d * d; }
#pragma unroll
  for (int o = 1; o < 16; o <<= 1) q2 += __shfl_xor(q2, o);
  float var = rbf(q2 * (1.0f / D_));
  float mb = rbf(mean);
  float den = rbf(sqrtf(rbf(var + 1e-5f)));
  float rden = 1.0f / den;
#pragma unroll
  for (int c = 0; c < 5; ++c) {
    u32x4 o;
#pragma unroll
    for (int e = 0; e < 4; ++e)
      o[e] = pack2(rbf(v[c * 8 + 2 * e] - mb) * rden, rbf(v[c * 8 + 2 * e + 1] - mb) * rden);
    *reinterpret_cast<u32x4*>(xn + (long)row * D_ + (c * 16 + li) * 8) = o;
  }
}

constexpr int NS1 = 3;
__global__ __launch_bounds__(512) void k_gemm1(const u16* __restrict__ xn, const u16* __restrict__ wqkv,
                                               const u16* __restrict__ w1, u16* __restrict__ QKV,
                                               u16* __restrict__ Hb) {
  __shared__ __attribute__((aligned(16))) char lds[NS1 * 49152];
  const int tile = (blockIdx.x & 7) * 27 + (blockIdx.x >> 3);
  const int bn = tile >> 3, bm = tile & 7;
  const int tid = threadIdx.x, lane = tid & 63;
  const int wid = __builtin_amdgcn_readfirstlane(tid >> 6);
  const int g = lane >> 4, li = lane & 15;
  const int wm = wid >> 1, wn = wid & 1;
  const u16* A = xn + (long)bm * 256 * D_;
  const bool isH = bn >= 15;
  const u16* Bp = isH ? (w1 + (bn - 15) * 128) : (wqkv + bn * 128);
  const int ldb = isH ? FF_ : NQKV_;
  f32x4 acc[4][4];
#pragma unroll
  for (int i = 0; i < 4; ++i)
#pragma unroll
    for (int j = 0; j < 4; ++j) acc[i][j] = f32x4{0.f, 0.f, 0.f, 0.f};
  gemm_loop<256, 128, 4, 2, NS1>(A, D_, Bp, ldb, D_ / 64, lds, acc);

  char* wl = lds + wid * (64 * 144);
  const float lo = isH ? 0.f : -3.0e38f;
#pragma unroll
  for (int mt = 0; mt < 4; ++mt)
#pragma unroll
    for (int nt = 0; nt < 4; ++nt) {
      f32x4 a = acc[mt][nt];
      u32x2 o = {pack2(fmaxf(a[0], lo), fmaxf(a[1], lo)), pack2(fmaxf(a[2], lo), fmaxf(a[3], lo))};
      *reinterpret_cast<u32x2*>(wl + (mt * 16 + li) * 144 + (nt * 16 + 4 * g) * 2) = o;
    }
  u16* C = isH ? (Hb + (bn - 15) * 128) : (QKV + bn * 128);
  C += (long)(bm * 256 + wm * 64) * ldb + wn * 64;
#pragma unroll
  for (int it = 0; it < 8; ++it) {
    int r = it * 8 + (lane >> 3), c = lane & 7;
    u32x4 v = *reinterpret_cast<const u32x4*>(wl + r * 144 + c * 16);
    *reinterpret_cast<u32x4*>(C + (long)r * ldb + c * 8) = v;
  }
}

__device__ __forceinline__ int kvsw(int row) { return (0x78 >> (2 * ((row >> 2) & 3))) & 3; }

__device__ __forceinline__ void attn_block(const u16* __restrict__ QKV, u16* __restrict__ O, char* lds, int h, int qblk) {
  char* Ks = lds;
  char* Vs = lds + 384 * 64;
  const int tid = threadIdx.x, lane = tid & 63;
  const int wid = __builtin_amdgcn_readfirstlane(tid >> 6);
  const int g = lane >> 4, li = lane & 15;
  const int kbase = qblk * 128 - 256;

  {
    u32x2 kv[12], vv[12];
#pragma unroll
    for (int i = 0; i < 12; ++i) {
      int idx = tid + 256 * i, kr = idx >> 3, sl = idx & 7;
      int key = kbase + kr;
      const bool ok = (sl < 5) && (key >= 0);
      const u16* src = QKV + (long)(key < 0 ? 0 : key) * NQKV_ + 640 + h * DH_ + (sl < 5 ? sl : 0) * 4;
      u32x2 a = *reinterpret_cast<const u32x2*>(src);
      u32x2 b = *reinterpret_cast<const u32x2*>(src + 640);
      kv[i] = u32x2{ok ? a[0] : 0u, ok ? a[1] : 0u};
      vv[i] = u32x2{ok ? b[0] : 0u, ok ? b[1] : 0u};
    }
#pragma unroll
    for (int i = 0; i < 12; ++i) {
      int idx = tid + 256 * i, kr = idx >> 3, sl = idx & 7;
      int off = kr * 64 + (((sl >> 1) ^ kvsw(kr)) << 4) + (sl & 1) * 8;
      *reinterpret_cast<u32x2*>(Ks + off) = kv[i];
      *reinterpret_cast<u32x2*>(Vs + off) = vv[i];
    }
  }
  const int q0 = qblk * 128 + wid * 32;
  bf16x8 qf[2];
#pragma unroll
  for (int a = 0; a < 2; ++a) {
    const u16* src = QKV + (long)(q0 + a * 16 + li) * NQKV_ + h * DH_;
    u32x2 x0 = *reinterpret_cast<const u32x2*>(src + (g < 3 ? g * 8 : 0));
    u32x2 x1 = *reinterpret_cast<const u32x2*>(src + (g < 2 ? g * 8 + 4 : 0));
    u32x4 t = {g < 3 ? x0[0] : 0u, g < 3 ? x0[1] : 0u, g < 2 ? x1[0] : 0u, g < 2 ? x1[1] : 0u};
    qf[a] = __builtin_bit_cast(bf16x8, t);
  }
  __syncthreads();

  const float C = 0.22360679774997896f * 1.4426950408889634f;
  const float NEG = -3e38f;
  const int lb = wid * 32;
  const int base = q0 - 256;
#pragma unroll 1
  for (int a = 0; a < 2; ++a) {
    const bf16x8 qa = a ? qf[1] : qf[0];
    const int q = q0 + a * 16 + li;
    f32x4 s[18];
#pragma unroll
    for (int t = 0; t < 18; ++t) {
      int r = lb + 16 * t + li;
      bf16x8 kf = *reinterpret_cast<const bf16x8*>(Ks + r * 64 + ((g ^ kvsw(r)) << 4));
      f32x4 z = {0.f, 0.f, 0.f, 0.f};
      s[t] = __builtin_amdgcn_mfma_f32_16x16x32_bf16(kf, qa, z, 0, 0, 0);
    }
#pragma unroll
    for (int t = 0; t < 18; ++t) {
      int key0 = base + 16 * t;
      if (t < 2 || t >= 16) {
#pragma unroll
        for (int e = 0; e < 4; ++e) {
          int key = key0 + 4 * g + e;
          bool ok = (key >= 0) && (key <= q) && (q - key < 256);
          s[t][e] = ok ? s[t][e] : NEG;
        }
      } else if (key0 < 0) {
        s[t] = f32x4{NEG, NEG, NEG, NEG};
      }
    }
    float mx = NEG;
#pragma unroll
    for (int t = 0; t < 18; ++t)
#pragma unroll
      for (int e = 0; e < 4; ++e) mx = fmaxf(mx, s[t][e]);
    mx = fmaxf(mx, __shfl_xor(mx, 16));
    mx = fmaxf(mx, __shfl_xor(mx, 32));
    const float mc = -mx * C;
    float sum = 0.f;
    f32x4 o0 = {0.f, 0.f, 0.f, 0.f}, o1 = {0.f, 0.f, 0.f, 0.f};
#pragma unroll
    for (int u = 0; u < 9; ++u) {
      bf16x8 vf[2];
#pragma unroll
      for (int dt = 0; dt < 2; ++dt) {
        int r0 = lb + 32 * u + 4 * g + (li >> 2), r1 = r0 + 16;
        int ch = dt * 2 + ((li & 3) >> 1), hb = (li & 1) * 8;
        s16x4 lo = __builtin_amdgcn_ds_read_tr16_b64_v4i16((lds_s16x4*)(Vs + r0 * 64 + ((ch ^ kvsw(r0)) << 4) + hb));
        s16x4 hi = __builtin_amdgcn_ds_read_tr16_b64_v4i16((lds_s16x4*)(Vs + r1 * 64 + ((ch ^ kvsw(r1)) << 4) + hb));
        vf[dt] = bf16x8{lo[0], lo[1], lo[2], lo[3], hi[0], hi[1], hi[2], hi[3]};
      }
      float p[8];
#pragma unroll
      for (int e = 0; e < 4; ++e) {
        p[e] = __builtin_amdgcn_exp2f(fmaf(s[2 * u][e], C, mc));
        p[4 + e] = __builtin_amdgcn_exp2f(fmaf(s[2 * u + 1][e], C, mc));
      }
#pragma unroll
      for (int e = 0; e < 8; ++e) sum += p[e];
      u32x4 pb = {pack2(p[0], p[1]), pack2(p[2], p[3]), pack2(p[4], p[5]), pack2(p[6], p[7])};
      bf16x8 pf = __builtin_bit_cast(bf16x8, pb);
      o0 = __builtin_amdgcn_mfma_f32_16x16x32_bf16(vf[0], pf, o0, 0, 0, 0);
      o1 = __builtin_amdgcn_mfma_f32_16x16x32_bf16(vf[1], pf, o1, 0, 0, 0);
    }
    sum += __shfl_xor(sum, 16);
    sum += __shfl_xor(sum, 32);
    float inv = 1.0f / sum;
    u16* dst = O + (long)q * D_ + h * DH_;
    u32x2 w0 = {pack2(o0[0] * inv, o0[1] * inv), pack2(o0[2] * inv, o0[3] * inv)};
    *reinterpret_cast<u32x2*>(dst + 4 * g) = w0;
    if (g == 0) {
      u32x2 w1 = {pack2(o1[0] * inv, o1[1] * inv), pack2(o1[2] * inv, o1[3] * inv)};
      *reinterpret_cast<u32x2*>(dst + 16) = w1;
    }
  }
}

__global__ __launch_bounds__(256, 2) void k_mid(const u16* __restrict__ QKV, u16* __restrict__ O,
                                                const u16* __restrict__ Hb, const u16* __restrict__ w2,
                                                u16* __restrict__ Fb) {
  __shared__ __attribute__((aligned(16))) char lds[3 * 24576];
  const int xcd = blockIdx.x & 7, loc = blockIdx.x >> 3;
  if (loc >= 20) {
    const int idx = loc - 20;
    attn_block(QKV, O, lds, xcd * 4 + (idx >> 4), idx & 15);
    return;
  }
  const int bm = xcd * 2 + (loc & 1);
  const int bn = loc >> 1;
  const int tid = threadIdx.x, lane = tid & 63;
  const int wid = __builtin_amdgcn_readfirstlane(tid >> 6);
  const int g = lane >> 4, li = lane & 15, wm = wid >> 1, wn = wid & 1;
  f32x4 acc[4][2];
#pragma unroll
  for (int i = 0; i < 4; ++i) { acc[i][0] = f32x4{0.f, 0.f, 0.f, 0.f}; acc[i][1] = f32x4{0.f, 0.f, 0.f, 0.f}; }
  gemm_loop<128, 64, 2, 2, 3>(Hb + (long)bm * 128 * FF_, FF_, w2 + bn * 64, D_, FF_ / 64, lds, acc);
#pragma unroll
  for (int mt = 0; mt < 4; ++mt)
#pragma unroll
    for (int nt = 0; nt < 2; ++nt) {
      int m = bm * 128 + wm * 64 + mt * 16 + li;
      int n = bn * 64 + wn * 32 + nt * 16 + 4 * g;
      u32x2 o = {pack2(acc[mt][nt][0], acc[mt][nt][1]), pack2(acc[mt][nt][2], acc[mt][nt][3])};
      *reinterpret_cast<u32x2*>(Fb + (long)m * D_ + n) = o;
    }
}

__global__ __launch_bounds__(256) void k_gemm2(const u16* __restrict__ x, const u16* __restrict__ O,
                                               const u16* __restrict__ wo, const u16* __restrict__ Fb,
                                               u16* __restrict__ out) {
  __shared__ __attribute__((aligned(16))) char lds[3 * 16384];
  const int loc = blockIdx.x >> 3;
  const int bm = (blockIdx.x & 7) * 4 + (loc & 3), bn = loc >> 2;
  const int tid = threadIdx.x, lane = tid & 63, wid = tid >> 6;
  const int g = lane >> 4, li = lane & 15, wm = wid >> 1, wn = wid & 1;
  u32x2 xv[2][2], fq[2][2];
#pragma unroll
  for (int mt = 0; mt < 2; ++mt)
#pragma unroll
    for (int nt = 0; nt < 2; ++nt) {
      int n = bn * 64 + wn * 32 + nt * 16 + 4 * g;
      int m = bm * 64 + wm * 32 + mt * 16 + li;
      long off = (long)m * D_ + n;
      xv[mt][nt] = *reinterpret_cast<const u32x2*>(x + off);
      fq[mt][nt] = *reinterpret_cast<const u32x2*>(Fb + off);
    }
  f32x4 as[2][2];
#pragma unroll
  for (int i = 0; i < 2; ++i)
#pragma unroll
    for (int j = 0; j < 2; ++j) as[i][j] = f32x4{0.f, 0.f, 0.f, 0.f};
  gemm_loop<64, 64, 2, 2, 3>(O + (long)bm * 64 * D_, D_, wo + bn * 64, D_, D_ / 64, lds, as);
#pragma unroll
  for (int mt = 0; mt < 2; ++mt)
#pragma unroll
    for (int nt = 0; nt < 2; ++nt) {
      int n = bn * 64 + wn * 32 + nt * 16 + 4 * g;
      int m = bm * 64 + wm * 32 + mt * 16 + li;
      u32x2 xq = xv[mt][nt];
      float xs[4] = {__uint_as_float(xq[0] << 16), __uint_as_float(xq[0] & 0xffff0000u),
                     __uint_as_float(xq[1] << 16), __uint_as_float(xq[1] & 0xffff0000u)};
      u32x2 fz = fq[mt][nt];
      float fs[4] = {__uint_as_float(fz[0] << 16), __uint_as_float(fz[0] & 0xffff0000u),
                     __uint_as_float(fz[1] << 16), __uint_as_float(fz[1] & 0xffff0000u)};
      float r[4];
#pragma unroll
      for (int e = 0; e < 4; ++e) {
        float sv = rbf(as[mt][nt][e]);
        float f = fs[e];
        float gg = rbf(f / (1.0f + __expf(-f)));
        r[e] = xs[e] + rbf(sv * gg);
      }
      u32x2 o = {pack2(r[0], r[1]), pack2(r[2], r[3])};
      *reinterpret_cast<u32x2*>(out + (long)m * D_ + n) = o;
    }
}

extern "C" void kernel_launch(void* const* d_in, const int* in_sizes, int n_in, void* d_out, int out_size,
                              void* d_ws, size_t ws_size, hipStream_t stream) {
  const u16* x = (const u16*)d_in[0];
  const u16* wqkv = (const u16*)d_in[1];
  const u16* wo = (const u16*)d_in[2];
  const u16* w1 = (const u16*)d_in[4];
  const u16* w2 = (const u16*)d_in[5];
  u16* out = (u16*)d_out;
  char* ws = (char*)d_ws;
  u16* QKV = (u16*)(ws);
  u16* Hb = (u16*)(ws + (8l << 20));
  u16* O = (u16*)(ws + (16l << 20));
  u16* xn = (u16*)(ws + (20l << 20));
  k_ln<<<128, 256, 0, stream>>>(x, xn);
  k_gemm1<<<8 * 27, 512, 0, stream>>>(xn, wqkv, w1, QKV, Hb);
  u16* Fb = (u16*)(ws + (24l << 20));
  k_mid<<<8 * 84, 256, 0, stream>>>(QKV, O, Hb, w2, Fb);
  k_gemm2<<<32 * 10, 256, 0, stream>>>(x, O, wo, Fb, out);
}
```
